```python
import torch

def _softmax(x, axis=-1):
    return torch.softmax(x, dim=axis)
def _gelu(x):
    return torch.nn.functional.gelu(x, approximate="tanh")
def _silu(x):
    return torch.nn.functional.silu(x)
def _sigmoid(x):
    return torch.sigmoid(x)
def _layernorm(x, axis=-1, eps=1e-5):
    m = x.mean(dim=axis, keepdim=True)
    v = x.var(dim=axis, keepdim=True, unbiased=False)
    return (x - m) / torch.sqrt(v + eps)
def _scatter_add(tgt, idx, src, axis=0):
    return tgt.clone().index_add_(axis, idx, src)
def _segment_sum(src, seg, n):
    out = torch.zeros((n,) + src.shape[1:], dtype=src.dtype)
    return out.index_add_(0, seg, src)
def _pad(x, pw):
    flat = tuple(v for pair in reversed(pw) for v in pair)
    return torch.nn.functional.pad(x, flat)

kernel_name = 'standalone__kv_cache_quant_int4_int2_kv_with_per_channel_or_per_token_scales__decode'

def setup_inputs():

    _w = (lambda a: (lambda t: t.to(torch.bfloat16) if t.is_floating_point() else t)(torch.as_tensor(a)))
    B = 2
    Sq = 1
    Skv = 32768
    Hq = 32
    Hkv = 4
    Dh = 128
    bits = 3
    G = 128
    if G <= 0 or G >= Skv:
        G = max(1, Skv // 2)
    Skv = (Skv // G) * G
    ngk = Skv // G
    qmax = (1 << bits) - 1
    return {
        "q": _w((lambda *s: torch.randn(*s, dtype=torch.bfloat16) * 1.6)(B, Hq, Sq, Dh)),
        "qk": _w(torch.randint(0, qmax + 1, (B, Hkv, Skv, Dh)).to(torch.int32)),
        "qv": _w(torch.randint(0, qmax + 1, (B, Hkv, Skv, Dh)).to(torch.int32)),
        "k_scales": _w(torch.abs((lambda *s: torch.randn(*s, dtype=torch.bfloat16) * 1.6)(B, Hkv, ngk, Dh)) * 0.05 + 0.02),
        "k_zeros": _w((lambda *s: torch.randn(*s, dtype=torch.bfloat16) * 1.6)(B, Hkv, ngk, Dh) + float(qmax) / 2.0),
        "v_scales": _w(torch.abs((lambda *s: torch.randn(*s, dtype=torch.bfloat16) * 0.05)(B, Hkv, Skv, 1)) * 0.05 + 0.02),
        "v_zeros": _w((lambda *s: torch.randn(*s, dtype=torch.bfloat16) * 0.05)(B, Hkv, Skv, 1) + float(qmax) / 2.0),
    }

def reference(q, qk, qv, k_scales, k_zeros, v_scales, v_zeros):
    B, Hq, Sq, Dh = q.shape
    Hkv, Skv = qk.shape[1], qk.shape[2]
    ngk = k_scales.shape[2]
    G = Skv // ngk
    ks = (lambda x, n, axis: torch.repeat_interleave(x, n, dim=axis))(k_scales, G, axis=2)
    kz = (lambda x, n, axis: torch.repeat_interleave(x, n, dim=axis))(k_zeros, G, axis=2)
    k = ((lambda x, dt: x.to(dt))(qk, q.dtype) - kz) * ks
    v = ((lambda x, dt: x.to(dt))(qv, q.dtype) - v_zeros) * v_scales
    rep = Hq // Hkv
    if rep > 1:
        k = (lambda x, n, axis: torch.repeat_interleave(x, n, dim=axis))(k, rep, axis=1)
        v = (lambda x, n, axis: torch.repeat_interleave(x, n, dim=axis))(v, rep, axis=1)
    scale = float(Dh) ** -0.5
    logits = torch.einsum("bhqd,bhkd->bhqk", q, k) * scale
    p = _softmax(logits, axis=-1)
    o = torch.einsum("bhqk,bhkd->bhqd", p, v)
    return o
```

```cpp
#include <hip/hip_runtime.h>
#include <hip/hip_bf16.h>
#include <stdint.h>

typedef int      i32x4 __attribute__((ext_vector_type(4)));
typedef unsigned u32x4 __attribute__((ext_vector_type(4)));
typedef unsigned u32x2 __attribute__((ext_vector_type(2)));
typedef float    f32x4 __attribute__((ext_vector_type(4)));
typedef float    f32x2 __attribute__((ext_vector_type(2)));
typedef __bf16   bf16x2 __attribute__((ext_vector_type(2)));
typedef __bf16   bf16x8 __attribute__((ext_vector_type(8)));

#define KV_AUX 2
#define WIN 24

constexpr int B = 2, HQ = 32, HKV = 4, SKV = 32768, D = 128, G = 128, NG = SKV / G, REP = HQ / HKV;
constexpr int NBH = B * HKV, NWAVE = 8, NSPLIT = NG / NWAVE;
constexpr int PART_F = 16 + REP * D;

__device__ __forceinline__ unsigned pk_bf16(float lo, float hi) {
  f32x2 v = {lo, hi};
  return __builtin_bit_cast(unsigned, __builtin_convertvector(v, bf16x2));
}
__device__ __forceinline__ float dot2(unsigned a, unsigned b) {
  return __builtin_amdgcn_fdot2_f32_bf16(__builtin_bit_cast(bf16x2, a), __builtin_bit_cast(bf16x2, b), 0.0f, false);
}
__device__ __forceinline__ float bf_lo(unsigned u) { return __builtin_bit_cast(float, u << 16); }
__device__ __forceinline__ float bf_hi(unsigned u) { return __builtin_bit_cast(float, u & 0xffff0000u); }
__device__ __forceinline__ float rnd_bf16(float x) { return bf_lo(pk_bf16(x, x)); }
template <int CTRL> __device__ __forceinline__ float dpp(float x) {
  return __builtin_bit_cast(float, __builtin_amdgcn_mov_dpp(__builtin_bit_cast(int, x), CTRL, 0xf, 0xf, true));
}
__device__ __forceinline__ float xrow_max(float x) {
  auto s = __builtin_amdgcn_permlane16_swap(__float_as_uint(x), __float_as_uint(x), false, false);
  x = fmaxf(__uint_as_float(s[0]), __uint_as_float(s[1]));
  auto t = __builtin_amdgcn_permlane32_swap(__float_as_uint(x), __float_as_uint(x), false, false);
  return fmaxf(__uint_as_float(t[0]), __uint_as_float(t[1]));
}
__device__ __forceinline__ float xrow_sum(float x) {
  auto s = __builtin_amdgcn_permlane16_swap(__float_as_uint(x), __float_as_uint(x), false, false);
  x = __uint_as_float(s[0]) + __uint_as_float(s[1]);
  auto t = __builtin_amdgcn_permlane32_swap(__float_as_uint(x), __float_as_uint(x), false, false);
  return __uint_as_float(t[0]) + __uint_as_float(t[1]);
}
#define SB() __builtin_amdgcn_sched_barrier(0)

__device__ __forceinline__ constexpr int k_off(int n) { int T = n >> 2, p = n & 3; return ((T >> 3) * 16 + (T & 7)) * 512 + p * 128; }
__device__ __forceinline__ constexpr int v_off(int n) { int Thp = n >> 4, Tl = (n >> 1) & 7, dh = n & 1; return (Thp * 8 + Tl) * 512 + dh * 256; }

__global__ __launch_bounds__(512, 2) void attn_main(const unsigned short* __restrict__ q, const int* __restrict__ qk, const int* __restrict__ qv,
                                                    const unsigned short* __restrict__ ksc, const unsigned short* __restrict__ kzr,
                                                    const unsigned short* __restrict__ vsc, const unsigned short* __restrict__ vzr,
                                                    float* __restrict__ part) {
  __shared__ __attribute__((aligned(16))) float lds[NWAVE * PART_F];
  const int tid = threadIdx.x, lane = tid & 63, wave = __builtin_amdgcn_readfirstlane(tid >> 6);
  const int wg = blockIdx.x, bh = wg / NSPLIT, split = wg % NSPLIT;
  const int grp = split * NWAVE + wave;
  const int i = lane & 15, g = lane >> 4, hf = i & 1;
  const size_t tokbase = (size_t)bh * SKV + (size_t)grp * G;
  auto rK = __builtin_amdgcn_make_buffer_rsrc((void*)(qk + tokbase * D), 0, G * D * 4, 0x00020000);
  auto rV = __builtin_amdgcn_make_buffer_rsrc((void*)(qv + tokbase * D), 0, G * D * 4, 0x00020000);
  const int voffK = (32 * (i >> 2) + 8 * ((i >> 1) & 1)) * 512 + (2 * g + hf) * 16;
  const int voffV = 32 * g * 512 + i * 16;

  u32x4 win[WIN];
#define ISSUE(n) do { if ((n) < 64) win[(n) % WIN] = __builtin_amdgcn_raw_buffer_load_b128(rK, voffK, k_off((n) & 63), KV_AUX); \
                      else if ((n) < 128) win[(n) % WIN] = __builtin_amdgcn_raw_buffer_load_b128(rV, voffV, v_off((n) & 63), KV_AUX); } while (0)
  u32x2 qf[4], ksr[4], kzq[4];
  {
    const unsigned short* qp = q + ((size_t)bh * REP + (i >> 1)) * D + (2 * g + hf) * 4;
    const size_t so = ((size_t)bh * NG + grp) * D + (2 * g + hf) * 4;
#pragma unroll
    for (int n = 0; n < 4; ++n) {
      qf[n] = *(const u32x2*)(qp + 32 * n);
      ksr[n] = *(const u32x2*)(ksc + so + 32 * n);
      kzq[n] = *(const u32x2*)(kzr + so + 32 * n);
    }
  }
  SB();
#pragma unroll
  for (int n = 0; n < WIN; ++n) ISSUE(n);
  SB();
  float kz[16]; unsigned ks[16];
#pragma unroll
  for (int n = 0; n < 4; ++n) {
    kz[4 * n + 0] = bf_lo(kzq[n][0]); kz[4 * n + 1] = bf_hi(kzq[n][0]); kz[4 * n + 2] = bf_lo(kzq[n][1]); kz[4 * n + 3] = bf_hi(kzq[n][1]);
    ks[4 * n + 0] = ksr[n][0] & 0xffffu; ks[4 * n + 1] = ksr[n][0] & 0xffff0000u; ks[4 * n + 2] = ksr[n][1] & 0xffffu; ks[4 * n + 3] = ksr[n][1] & 0xffff0000u;
  }
  u32x4 qb[2] = {{qf[0][0], qf[0][1], qf[1][0], qf[1][1]}, {qf[2][0], qf[2][1], qf[3][0], qf[3][1]}};
  u32x4 vsr[4], vzq[4];
  float s[16];
#pragma unroll
  for (int T = 0; T < 16; ++T) {
    f32x4 d = {0.f, 0.f, 0.f, 0.f};
#pragma unroll
    for (int m = 0; m < 2; ++m) {
      u32x4 a;
#pragma unroll
      for (int u = 0; u < 2; ++u) {
        const int p = 2 * m + u, n = 4 * T + p;
        const u32x4 x = win[n % WIN];
        const unsigned d01 = pk_bf16((float)(int)x[0] - kz[4 * p + 0], (float)(int)x[1] - kz[4 * p + 1]);
        const unsigned d23 = pk_bf16((float)(int)x[2] - kz[4 * p + 2], (float)(int)x[3] - kz[4 * p + 3]);
        a[2 * u + 0] = pk_bf16(dot2(d01, ks[4 * p + 0]), dot2(d01, ks[4 * p + 1]));
        a[2 * u + 1] = pk_bf16(dot2(d23, ks[4 * p + 2]), dot2(d23, ks[4 * p + 3]));
        SB();
        if (n + WIN == 64) {
          const size_t vo = tokbase + 32 * g;
#pragma unroll
          for (int k = 0; k < 4; ++k) { vsr[k] = *(const u32x4*)(vsc + vo + 8 * k); vzq[k] = *(const u32x4*)(vzr + vo + 8 * k); }
        }
        ISSUE(n + WIN);
        SB();
      }
      d = __builtin_amdgcn_mfma_f32_16x16x32_bf16(__builtin_bit_cast(bf16x8, a), __builtin_bit_cast(bf16x8, qb[m]), d, 0, 0, 0);
    }
    const float mine = hf ? d[3] : d[0], send = hf ? d[1] : d[2];
    const float dot = mine + dpp<0xB1>(send);
    s[T] = rnd_bf16(rnd_bf16(dot) * 0.088388347648318440f);
  }
  float mx = s[0];
#pragma unroll
  for (int T = 1; T < 16; ++T) mx = fmaxf(mx, s[T]);
  mx = fmaxf(mx, dpp<0xB1>(mx));
  mx = xrow_max(mx);
  float lsum = 0.f;
  constexpr float LOG2E = 1.4426950408889634f;
#pragma unroll
  for (int T = 0; T < 16; ++T) { s[T] = __builtin_amdgcn_exp2f((s[T] - mx) * LOG2E); lsum += s[T]; }
  u32x4 pa[4];
#pragma unroll
  for (int Th = 0; Th < 2; ++Th)
#pragma unroll
    for (int k = 0; k < 4; ++k) {
      const unsigned pp = pk_bf16(s[8 * Th + 2 * k], s[8 * Th + 2 * k + 1]);
      pa[2 * Th + 0][k] = hf ? 0u : pp;
      pa[2 * Th + 1][k] = hf ? pp : 0u;
    }
  lsum += dpp<0xB1>(lsum);
  lsum = xrow_sum(lsum);
  f32x4 acc[8];
#pragma unroll
  for (int k = 0; k < 8; ++k) acc[k] = f32x4{0.f, 0.f, 0.f, 0.f};
#pragma unroll
  for (int Thp = 0; Thp < 4; ++Thp) {
    u32x4 bv[8];
#pragma unroll
    for (int tp = 0; tp < 4; ++tp) {
      const float z0 = bf_lo(vzq[Thp][tp]), z1 = bf_hi(vzq[Thp][tp]);
      const unsigned s0 = vsr[Thp][tp] & 0xffffu, s1 = vsr[Thp][tp] & 0xffff0000u;
      const int n0 = 64 + Thp * 16 + tp * 4;
#pragma unroll
      for (int dh = 0; dh < 2; ++dh) {
        const u32x4 x0 = win[(n0 + dh) % WIN], x1 = win[(n0 + 2 + dh) % WIN];
#pragma unroll
        for (int c = 0; c < 4; ++c) {
          const unsigned dd = pk_bf16((float)(int)x0[c] - z0, (float)(int)x1[c] - z1);
          bv[dh * 4 + c][tp] = pk_bf16(dot2(dd, s0), dot2(dd, s1));
        }
      }
      SB();
#pragma unroll
      for (int u = 0; u < 4; ++u) ISSUE(n0 + u + WIN);
      SB();
    }
#pragma unroll
    for (int k = 0; k < 8; ++k)
      acc[k] = __builtin_amdgcn_mfma_f32_16x16x32_bf16(__builtin_bit_cast(bf16x8, pa[Thp]), __builtin_bit_cast(bf16x8, bv[k]), acc[k], 0, 0, 0);
  }
  float* my = lds + wave * PART_F;
#pragma unroll
  for (int u = 0; u < 2; ++u)
#pragma unroll
    for (int dh = 0; dh < 2; ++dh) {
      f32x4 o;
#pragma unroll
      for (int c = 0; c < 4; ++c) o[c] = acc[dh * 4 + c][2 * u] + acc[dh * 4 + c][2 * u + 1];
      *(f32x4*)(my + 16 + (2 * g + u) * D + 64 * dh + 4 * i) = o;
    }
  if (lane < 16 && hf == 0) { my[i >> 1] = mx; my[8 + (i >> 1)] = lsum; }
  __syncthreads();
  {
    const int h = wave, d0 = 2 * lane;
    float mw[NWAVE], M = -INFINITY;
#pragma unroll
    for (int w = 0; w < NWAVE; ++w) { mw[w] = lds[w * PART_F + h]; M = fmaxf(M, mw[w]); }
    float L = 0.f, o0 = 0.f, o1 = 0.f;
#pragma unroll
    for (int w = 0; w < NWAVE; ++w) {
      const float e = __builtin_amdgcn_exp2f((mw[w] - M) * LOG2E);
      const f32x2 o = *(const f32x2*)(lds + w * PART_F + 16 + h * D + d0);
      L = fmaf(e, lds[w * PART_F + 8 + h], L); o0 = fmaf(e, o[0], o0); o1 = fmaf(e, o[1], o1);
    }
    float* pp = part + (size_t)wg * PART_F;
    *(f32x2*)(pp + 16 + h * D + d0) = f32x2{o0, o1};
    if (lane == 0) { pp[h] = M; pp[8 + h] = L; }
  }
}

__global__ __launch_bounds__(128) void attn_combine(const float* __restrict__ part, __hip_bfloat16* __restrict__ out) {
  const int row = blockIdx.x, bh = row / REP, h = row % REP, d = threadIdx.x;
  const float* pp = part + (size_t)bh * NSPLIT * PART_F;
  float m[NSPLIT], l[NSPLIT], o[NSPLIT];
#pragma unroll
  for (int s = 0; s < NSPLIT; ++s) { m[s] = pp[s * PART_F + h]; l[s] = pp[s * PART_F + 8 + h]; o[s] = pp[s * PART_F + 16 + h * D + d]; }
  float M = -INFINITY;
#pragma unroll
  for (int s = 0; s < NSPLIT; ++s) M = fmaxf(M, m[s]);
  float L = 0.f, O = 0.f;
#pragma unroll
  for (int s = 0; s < NSPLIT; ++s) { const float e = __builtin_amdgcn_exp2f((m[s] - M) * 1.4426950408889634f); L = fmaf(e, l[s], L); O = fmaf(e, o[s], O); }
  out[row * D + d] = __float2bfloat16(O / L);
}

extern "C" void kernel_launch(void* const* d_in, const int* in_sizes, int n_in, void* d_out, int out_size, void* d_ws, size_t ws_size,
                              hipStream_t stream) {
  float* part = (float*)d_ws;
  attn_main<<<NBH * NSPLIT, 512, 0, stream>>>((const unsigned short*)d_in[0], (const int*)d_in[1], (const int*)d_in[2],
                                              (const unsigned short*)d_in[3], (const unsigned short*)d_in[4],
                                              (const unsigned short*)d_in[5], (const unsigned short*)d_in[6], part);
  attn_combine<<<B * HQ, 128, 0, stream>>>(part, (__hip_bfloat16*)d_out);
}
```

```cpp
#include <hip/hip_runtime.h>
#include <hip/hip_bf16.h>
#include <stdint.h>

typedef unsigned u32x4 __attribute__((ext_vector_type(4)));
typedef unsigned u32x2 __attribute__((ext_vector_type(2)));
typedef float    f32x4 __attribute__((ext_vector_type(4)));
typedef float    f32x2 __attribute__((ext_vector_type(2)));
typedef __bf16   bf16x2 __attribute__((ext_vector_type(2)));
typedef __bf16   bf16x8 __attribute__((ext_vector_type(8)));
typedef short    s16x4 __attribute__((ext_vector_type(4)));

#define KV_AUX 2
#define WIN 32
#define MAP 1

constexpr int B = 2, HQ = 32, HKV = 4, SKV = 32768, D = 128, G = 128, NG = SKV / G, REP = HQ / HKV;
constexpr int NBH = B * HKV, NWAVE = 8, NSPLIT = 32, NSTEP = NG / NSPLIT;
constexpr int PART_F = 16 + REP * D;
constexpr int STEP_BYTES = NSPLIT * G * D * 4;
constexpr float LOG2E = 1.4426950408889634f;

__device__ __forceinline__ unsigned pk_bf16(float lo, float hi) {
  f32x2 v = {lo, hi};
  return __builtin_bit_cast(unsigned, __builtin_convertvector(v, bf16x2));
}
__device__ __forceinline__ float bf_lo(unsigned u) { return __builtin_bit_cast(float, u << 16); }
__device__ __forceinline__ float bf_hi(unsigned u) { return __builtin_bit_cast(float, u & 0xffff0000u); }
__device__ __forceinline__ float rnd_bf16(float x) { return __builtin_bit_cast(float, pk_bf16(0.f, x)); }
template <int CTRL> __device__ __forceinline__ float dpp(float x) {
  return __builtin_bit_cast(float, __builtin_amdgcn_mov_dpp(__builtin_bit_cast(int, x), CTRL, 0xf, 0xf, true));
}
__device__ __forceinline__ float xrow_max(float x) {
  auto s = __builtin_amdgcn_permlane16_swap(__float_as_uint(x), __float_as_uint(x), false, false);
  x = fmaxf(__uint_as_float(s[0]), __uint_as_float(s[1]));
  auto t = __builtin_amdgcn_permlane32_swap(__float_as_uint(x), __float_as_uint(x), false, false);
  return fmaxf(__uint_as_float(t[0]), __uint_as_float(t[1]));
}
__device__ __forceinline__ float xrow_sum(float x) {
  auto s = __builtin_amdgcn_permlane16_swap(__float_as_uint(x), __float_as_uint(x), false, false);
  x = __uint_as_float(s[0]) + __uint_as_float(s[1]);
  auto t = __builtin_amdgcn_permlane32_swap(__float_as_uint(x), __float_as_uint(x), false, false);
  return __uint_as_float(t[0]) + __uint_as_float(t[1]);
}
__device__ __forceinline__ unsigned deq2(unsigned c0, unsigned c1, f32x2 z, f32x2 s) {
  f32x2 x = {(float)(int)c0, (float)(int)c1};
  x -= z;
  f32x2 r = {rnd_bf16(x[0]), rnd_bf16(x[1])};
  r *= s;
  return pk_bf16(r[0], r[1]);
}
#define SB() __builtin_amdgcn_sched_barrier(0)

__global__ __launch_bounds__(512, 2) void attn_main(const unsigned short* __restrict__ q, const int* __restrict__ qk, const int* __restrict__ qv,
                                                    const unsigned short* __restrict__ ksc, const unsigned short* __restrict__ kzr,
                                                    const unsigned short* __restrict__ vsc, const unsigned short* __restrict__ vzr,
                                                    float* __restrict__ part) {
  __shared__ __attribute__((aligned(16))) float lds[4 * NSTEP * 128 + NWAVE * PART_F];
  const int tid = threadIdx.x, lane = tid & 63, wave = __builtin_amdgcn_readfirstlane(tid >> 6);
  const int wg = blockIdx.x;
  const int bh = MAP ? wg % NBH : wg / NSPLIT, split = MAP ? wg / NBH : wg % NSPLIT;
  const int i = lane & 15, g = lane >> 4, hf = i & 1;
  const size_t tok0 = (size_t)bh * SKV + split * G + wave * 16;
  auto rK = __builtin_amdgcn_make_buffer_rsrc((void*)(qk + tok0 * D), 0, (NSTEP - 1) * STEP_BYTES + 16 * D * 4, 0x00020000);
  auto rV = __builtin_amdgcn_make_buffer_rsrc((void*)(qv + tok0 * D), 0, (NSTEP - 1) * STEP_BYTES + 16 * D * 4, 0x00020000);
  const int voffK = (4 * (i >> 2) + ((i >> 1) & 1)) * 512 + (2 * g + hf) * 16;
  const int voffV = 4 * g * 512 + i * 16;

  u32x4 win[WIN];
#define ISSUE(n) do { if ((n) < 64) win[(n) % WIN] = __builtin_amdgcn_raw_buffer_load_b128(rK, voffK + (((n) >> 2) & 1) * 1024 + ((n) & 3) * 128, ((n) >> 3) * STEP_BYTES, KV_AUX); \
                      else if ((n) < 128) win[(n) % WIN] = __builtin_amdgcn_raw_buffer_load_b128(rV, voffV + ((n) & 7) * 256, (((n) - 64) >> 3) * STEP_BYTES, KV_AUX); } while (0)
  u32x2 qf[4], st0, st1;
  {
    const unsigned short* qp = q + ((size_t)bh * REP + (i >> 1)) * D + (2 * g + hf) * 4;
    const int arr = wave >> 1, stp = (wave & 1) * 4 + (lane >> 5) * 2;
    const unsigned short* src = arr == 0 ? ksc : arr == 1 ? kzr : arr == 2 ? vsc : vzr;
    src += ((size_t)bh * NG + split + stp * NSPLIT) * 128 + (lane & 31) * 4;
    st0 = *(const u32x2*)src;
    st1 = *(const u32x2*)(src + NSPLIT * 128);
#pragma unroll
    for (int n = 0; n < 4; ++n) qf[n] = *(const u32x2*)(qp + 32 * n);
    SB();
#pragma unroll
    for (int n = 0; n < WIN; ++n) { ISSUE(n); SB(); }
    float* dst = lds + (arr * NSTEP + stp) * 128 + (lane & 31) * 4;
    *(f32x4*)dst = f32x4{bf_lo(st0[0]), bf_hi(st0[0]), bf_lo(st0[1]), bf_hi(st0[1])};
    *(f32x4*)(dst + 128) = f32x4{bf_lo(st1[0]), bf_hi(st1[0]), bf_lo(st1[1]), bf_hi(st1[1])};
  }
  __syncthreads();
  u32x4 qb[2] = {{qf[0][0], qf[0][1], qf[1][0], qf[1][1]}, {qf[2][0], qf[2][1], qf[3][0], qf[3][1]}};
  float s[16];
  const float* kc = lds + (2 * g + hf) * 4;
#pragma unroll
  for (int st = 0; st < NSTEP; ++st) {
    f32x4 ks[4], kz[4];
#pragma unroll
    for (int p = 0; p < 4; ++p) { ks[p] = *(const f32x4*)(kc + st * 128 + p * 32); kz[p] = *(const f32x4*)(kc + (NSTEP + st) * 128 + p * 32); }
#pragma unroll
    for (int u = 0; u < 2; ++u) {
      f32x4 d = {0.f, 0.f, 0.f, 0.f};
#pragma unroll
      for (int m = 0; m < 2; ++m) {
        u32x4 a;
#pragma unroll
        for (int e = 0; e < 2; ++e) {
          const int p = 2 * m + e, n = 8 * st + 4 * u + p;
          const u32x4 x = win[n % WIN];
          a[2 * e + 0] = deq2(x[0], x[1], f32x2{kz[p][0], kz[p][1]}, f32x2{ks[p][0], ks[p][1]});
          a[2 * e + 1] = deq2(x[2], x[3], f32x2{kz[p][2], kz[p][3]}, f32x2{ks[p][2], ks[p][3]});
          SB();
          ISSUE(n + WIN);
          SB();
        }
        d = __builtin_amdgcn_mfma_f32_16x16x32_bf16(__builtin_bit_cast(bf16x8, a), __builtin_bit_cast(bf16x8, qb[m]), d, 0, 0, 0);
      }
      const float mine = hf ? d[3] : d[0], send = hf ? d[1] : d[2];
      const float dot = mine + dpp<0xB1>(send);
      s[2 * st + u] = rnd_bf16(rnd_bf16(dot) * 0.088388347648318440f);
    }
  }
  float mx = s[0];
#pragma unroll
  for (int T = 1; T < 16; ++T) mx = fmaxf(mx, s[T]);
  mx = fmaxf(mx, dpp<0xB1>(mx));
  mx = xrow_max(mx);
  float lsum = 0.f;
  unsigned pa[16];
#pragma unroll
  for (int T = 0; T < 16; ++T) {
    const float p = __builtin_amdgcn_exp2f((s[T] - mx) * LOG2E);
    lsum += p;
    pa[T] = pk_bf16(hf ? 0.f : p, hf ? p : 0.f);
  }
  lsum += dpp<0xB1>(lsum);
  lsum = xrow_sum(lsum);
  f32x4 acc[8];
#pragma unroll
  for (int k = 0; k < 8; ++k) acc[k] = f32x4{0.f, 0.f, 0.f, 0.f};
  const float* vc = lds + 2 * NSTEP * 128 + wave * 16 + 4 * g;
#pragma unroll
  for (int st = 0; st < NSTEP; ++st) {
    const f32x4 vs = *(const f32x4*)(vc + st * 128), vz = *(const f32x4*)(vc + (NSTEP + st) * 128);
    u32x2 bv[8];
#pragma unroll
    for (int u = 0; u < 2; ++u) {
      const int n0 = 64 + 8 * st + 4 * u;
#pragma unroll
      for (int dh = 0; dh < 2; ++dh) {
        const u32x4 x0 = win[(n0 + dh) % WIN], x1 = win[(n0 + 2 + dh) % WIN];
#pragma unroll
        for (int c = 0; c < 4; ++c) bv[dh * 4 + c][u] = deq2(x0[c], x1[c], f32x2{vz[2 * u], vz[2 * u + 1]}, f32x2{vs[2 * u], vs[2 * u + 1]});
      }
      SB();
#pragma unroll
      for (int e = 0; e < 4; ++e) { ISSUE(n0 + e + WIN); SB(); }
    }
    const u32x2 pp = {pa[2 * st], pa[2 * st + 1]};
#pragma unroll
    for (int k = 0; k < 8; ++k)
      acc[k] = __builtin_amdgcn_mfma_f32_16x16x16bf16_1k(__builtin_bit_cast(s16x4, pp), __builtin_bit_cast(s16x4, bv[k]), acc[k], 0, 0, 0);
  }
  float* mrg = lds + 4 * NSTEP * 128;
  float* my = mrg + wave * PART_F;
#pragma unroll
  for (int u = 0; u < 2; ++u)
#pragma unroll
    for (int dh = 0; dh < 2; ++dh) {
      f32x4 o;
#pragma unroll
      for (int c = 0; c < 4; ++c) o[c] = acc[dh * 4 + c][2 * u] + acc[dh * 4 + c][2 * u + 1];
      *(f32x4*)(my + 16 + (2 * g + u) * D + 64 * dh + 4 * i) = o;
    }
  if (lane < 16 && hf == 0) { my[i >> 1] = mx; my[8 + (i >> 1)] = lsum; }
  __syncthreads();
  {
    const int h = wave, d0 = 2 * lane;
    float mw[NWAVE], M = -INFINITY;
#pragma unroll
    for (int w = 0; w < NWAVE; ++w) { mw[w] = mrg[w * PART_F + h]; M = fmaxf(M, mw[w]); }
    float L = 0.f, o0 = 0.f, o1 = 0.f;
#pragma unroll
    for (int w = 0; w < NWAVE; ++w) {
      const float e = __builtin_amdgcn_exp2f((mw[w] - M) * LOG2E);
      const f32x2 o = *(const f32x2*)(mrg + w * PART_F + 16 + h * D + d0);
      L = fmaf(e, mrg[w * PART_F + 8 + h], L); o0 = fmaf(e, o[0], o0); o1 = fmaf(e, o[1], o1);
    }
    float* pp = part + (size_t)(bh * NSPLIT + split) * PART_F;
    *(f32x2*)(pp + 16 + h * D + d0) = f32x2{o0, o1};
    if (lane == 0) { pp[h] = M; pp[8 + h] = L; }
  }
}

__global__ __launch_bounds__(128) void attn_combine(const float* __restrict__ part, __hip_bfloat16* __restrict__ out) {
  const int row = blockIdx.x, bh = row / REP, h = row % REP, d = threadIdx.x;
  const float* pp = part + (size_t)bh * NSPLIT * PART_F;
  float m[NSPLIT], l[NSPLIT], o[NSPLIT];
#pragma unroll
  for (int s = 0; s < NSPLIT; ++s) { m[s] = pp[s * PART_F + h]; l[s] = pp[s * PART_F + 8 + h]; o[s] = pp[s * PART_F + 16 + h * D + d]; }
  float M = -INFINITY;
#pragma unroll
  for (int s = 0; s < NSPLIT; ++s) M = fmaxf(M, m[s]);
  float L = 0.f, O = 0.f;
#pragma unroll
  for (int s = 0; s < NSPLIT; ++s) { const float e = __builtin_amdgcn_exp2f((m[s] - M) * LOG2E); L = fmaf(e, l[s], L); O = fmaf(e, o[s], O); }
  out[row * D + d] = __float2bfloat16(O / L);
}

extern "C" void kernel_launch(void* const* d_in, const int* in_sizes, int n_in, void* d_out, int out_size, void* d_ws, size_t ws_size,
                              hipStream_t stream) {
  float* part = (float*)d_ws;
  attn_main<<<NBH * NSPLIT, 512, 0, stream>>>((const unsigned short*)d_in[0], (const int*)d_in[1], (const int*)d_in[2],
                                              (const unsigned short*)d_in[3], (const unsigned short*)d_in[4],
                                              (const unsigned short*)d_in[5], (const unsigned short*)d_in[6], part);
  attn_combine<<<B * HQ, 128, 0, stream>>>(part, (__hip_bfloat16*)d_out);
}
```

```cpp
#include <hip/hip_runtime.h>
#include <hip/hip_bf16.h>
#include <stdint.h>
#include <stdio.h>

typedef unsigned u32x4 __attribute__((ext_vector_type(4)));
typedef unsigned u32x2 __attribute__((ext_vector_type(2)));
typedef float    f32x4 __attribute__((ext_vector_type(4)));
typedef float    f32x2 __attribute__((ext_vector_type(2)));
typedef __bf16   bf16x2 __attribute__((ext_vector_type(2)));
typedef __bf16   bf16x8 __attribute__((ext_vector_type(8)));
typedef short    s16x4 __attribute__((ext_vector_type(4)));

#define KV_AUX 2
#define WIN 16
#define MAP 1

constexpr int B = 2, HQ = 32, HKV = 4, SKV = 32768, D = 128, G = 128, NG = SKV / G, REP = HQ / HKV;
constexpr int NBH = B * HKV, NWAVE = 8, NSPLIT = 32, NSTEP = NG / NSPLIT;
constexpr int PART_F = 16 + REP * D;
constexpr int STEP_BYTES = NSPLIT * G * D * 4;
constexpr float LOG2E = 1.4426950408889634f;

__device__ __forceinline__ unsigned pk_bf16(float lo, float hi) {
  f32x2 v = {lo, hi};
  return __builtin_bit_cast(unsigned, __builtin_convertvector(v, bf16x2));
}
__device__ __forceinline__ float bf_lo(unsigned u) { return __builtin_bit_cast(float, u << 16); }
__device__ __forceinline__ float bf_hi(unsigned u) { return __builtin_bit_cast(float, u & 0xffff0000u); }
__device__ __forceinline__ float rnd_bf16(float x) { return __builtin_bit_cast(float, pk_bf16(0.f, x)); }
template <int CTRL> __device__ __forceinline__ float dpp(float x) {
  return __builtin_bit_cast(float, __builtin_amdgcn_mov_dpp(__builtin_bit_cast(int, x), CTRL, 0xf, 0xf, true));
}
__device__ __forceinline__ float xrow_max(float x) {
  auto s = __builtin_amdgcn_permlane16_swap(__float_as_uint(x), __float_as_uint(x), false, false);
  x = fmaxf(__uint_as_float(s[0]), __uint_as_float(s[1]));
  auto t = __builtin_amdgcn_permlane32_swap(__float_as_uint(x), __float_as_uint(x), false, false);
  return fmaxf(__uint_as_float(t[0]), __uint_as_float(t[1]));
}
__device__ __forceinline__ float xrow_sum(float x) {
  auto s = __builtin_amdgcn_permlane16_swap(__float_as_uint(x), __float_as_uint(x), false, false);
  x = __uint_as_float(s[0]) + __uint_as_float(s[1]);
  auto t = __builtin_amdgcn_permlane32_swap(__float_as_uint(x), __float_as_uint(x), false, false);
  return __uint_as_float(t[0]) + __uint_as_float(t[1]);
}
__device__ __forceinline__ unsigned deq2(unsigned c0, unsigned c1, f32x2 z, f32x2 s) {
  f32x2 x = {(float)(int)c0, (float)(int)c1};
  x -= z;
  f32x2 r = {rnd_bf16(x[0]), rnd_bf16(x[1])};
  r *= s;
  return pk_bf16(r[0], r[1]);
}
#define SB() __builtin_amdgcn_sched_barrier(0)

__global__ __launch_bounds__(512, 2) void attn_main(const unsigned short* __restrict__ q, const int* __restrict__ qk, const int* __restrict__ qv,
                                                    const unsigned short* __restrict__ ksc, const unsigned short* __restrict__ kzr,
                                                    const unsigned short* __restrict__ vsc, const unsigned short* __restrict__ vzr,
                                                    float* __restrict__ part) {
  __shared__ __attribute__((aligned(16))) float lds[4 * NSTEP * 128 + NWAVE * PART_F];
  const int tid = threadIdx.x, lane = tid & 63, wave = __builtin_amdgcn_readfirstlane(tid >> 6);
  const int wg = blockIdx.x;
  const int bh = MAP ? wg % NBH : wg / NSPLIT, split = MAP ? wg / NBH : wg % NSPLIT;
  const int i = lane & 15, g = lane >> 4, hf = i & 1;
  const size_t tok0 = (size_t)bh * SKV + split * G + wave * 16;
  auto rK = __builtin_amdgcn_make_buffer_rsrc((void*)(qk + tok0 * D), 0, (NSTEP - 1) * STEP_BYTES + 16 * D * 4, 0x00020000);
  auto rV = __builtin_amdgcn_make_buffer_rsrc((void*)(qv + tok0 * D), 0, (NSTEP - 1) * STEP_BYTES + 16 * D * 4, 0x00020000);
  const int voffK = (4 * (i >> 2) + ((i >> 1) & 1)) * 512 + (2 * g + hf) * 16;
  const int voffV = 4 * g * 512 + i * 16;

  u32x4 win[WIN];
#define ISSUE(n) do { if ((n) < 64) win[(n) % WIN] = __builtin_amdgcn_raw_buffer_load_b128(rK, voffK + (((n) >> 2) & 1) * 1024 + ((n) & 3) * 128, ((n) >> 3) * STEP_BYTES, KV_AUX); \
                      else if ((n) < 128) win[(n) % WIN] = __builtin_amdgcn_raw_buffer_load_b128(rV, voffV + ((n) & 7) * 256, (((n) - 64) >> 3) * STEP_BYTES, KV_AUX); } while (0)
  u32x2 qf[4], st0, st1;
  {
    const unsigned short* qp = q + ((size_t)bh * REP + (i >> 1)) * D + (2 * g + hf) * 4;
    const int arr = wave >> 1, stp = (wave & 1) * 4 + (lane >> 5) * 2;
    const unsigned short* src = arr == 0 ? ksc : arr == 1 ? kzr : arr == 2 ? vsc : vzr;
    src += ((size_t)bh * NG + split + stp * NSPLIT) * 128 + (lane & 31) * 4;
    st0 = *(const u32x2*)src;
    st1 = *(const u32x2*)(src + NSPLIT * 128);
#pragma unroll
    for (int n = 0; n < 4; ++n) qf[n] = *(const u32x2*)(qp + 32 * n);
    SB();
#pragma unroll
    for (int n = 0; n < WIN; ++n) { ISSUE(n); SB(); }
    float* dst = lds + (arr * NSTEP + stp) * 128 + (lane & 31) * 4;
    *(f32x4*)dst = f32x4{bf_lo(st0[0]), bf_hi(st0[0]), bf_lo(st0[1]), bf_hi(st0[1])};
    *(f32x4*)(dst + 128) = f32x4{bf_lo(st1[0]), bf_hi(st1[0]), bf_lo(st1[1]), bf_hi(st1[1])};
  }
  __syncthreads();
  u32x4 qb[2] = {{qf[0][0], qf[0][1], qf[1][0], qf[1][1]}, {qf[2][0], qf[2][1], qf[3][0], qf[3][1]}};
  float s[16];
  const float* kc = lds + (2 * g + hf) * 4;
#pragma unroll
  for (int st = 0; st < NSTEP; ++st) {
    f32x4 ks[4], kz[4];
#pragma unroll
    for (int p = 0; p < 4; ++p) { ks[p] = *(const f32x4*)(kc + st * 128 + p * 32); kz[p] = *(const f32x4*)(kc + (NSTEP + st) * 128 + p * 32); }
#pragma unroll
    for (int u = 0; u < 2; ++u) {
      f32x4 d = {0.f, 0.f, 0.f, 0.f};
#pragma unroll
      for (int m = 0; m < 2; ++m) {
        u32x4 a;
#pragma unroll
        for (int e = 0; e < 2; ++e) {
          const int p = 2 * m + e, n = 8 * st + 4 * u + p;
          const u32x4 x = win[n % WIN];
          a[2 * e + 0] = deq2(x[0], x[1], f32x2{kz[p][0], kz[p][1]}, f32x2{ks[p][0], ks[p][1]});
          a[2 * e + 1] = deq2(x[2], x[3], f32x2{kz[p][2], kz[p][3]}, f32x2{ks[p][2], ks[p][3]});
          SB();
          ISSUE(n + WIN);
          SB();
        }
        d = __builtin_amdgcn_mfma_f32_16x16x32_bf16(__builtin_bit_cast(bf16x8, a), __builtin_bit_cast(bf16x8, qb[m]), d, 0, 0, 0);
      }
      const float mine = hf ? d[3] : d[0], send = hf ? d[1] : d[2];
      const float dot = mine + dpp<0xB1>(send);
      s[2 * st + u] = rnd_bf16(rnd_bf16(dot) * 0.088388347648318440f);
    }
  }
  float mx = s[0];
#pragma unroll
  for (int T = 1; T < 16; ++T) mx = fmaxf(mx, s[T]);
  mx = fmaxf(mx, dpp<0xB1>(mx));
  mx = xrow_max(mx);
  float lsum = 0.f;
  unsigned pa[16];
#pragma unroll
  for (int T = 0; T < 16; ++T) {
    const float p = __builtin_amdgcn_exp2f((s[T] - mx) * LOG2E);
    lsum += p;
    pa[T] = pk_bf16(hf ? 0.f : p, hf ? p : 0.f);
  }
  lsum += dpp<0xB1>(lsum);
  lsum = xrow_sum(lsum);
  f32x4 acc[8];
#pragma unroll
  for (int k = 0; k < 8; ++k) acc[k] = f32x4{0.f, 0.f, 0.f, 0.f};
  const float* vc = lds + 2 * NSTEP * 128 + wave * 16 + 4 * g;
#pragma unroll
  for (int st = 0; st < NSTEP; ++st) {
    const f32x4 vs = *(const f32x4*)(vc + st * 128), vz = *(const f32x4*)(vc + (NSTEP + st) * 128);
    u32x2 bv[8];
#pragma unroll
    for (int u = 0; u < 2; ++u) {
      const int n0 = 64 + 8 * st + 4 * u;
#pragma unroll
      for (int dh = 0; dh < 2; ++dh) {
        const u32x4 x0 = win[(n0 + dh) % WIN], x1 = win[(n0 + 2 + dh) % WIN];
#pragma unroll
        for (int c = 0; c < 4; ++c) bv[dh * 4 + c][u] = deq2(x0[c], x1[c], f32x2{vz[2 * u], vz[2 * u + 1]}, f32x2{vs[2 * u], vs[2 * u + 1]});
      }
      SB();
#pragma unroll
      for (int e = 0; e < 4; ++e) { ISSUE(n0 + e + WIN); SB(); }
    }
    const u32x2 pp = {pa[2 * st], pa[2 * st + 1]};
#pragma unroll
    for (int k = 0; k < 8; ++k)
      acc[k] = __builtin_amdgcn_mfma_f32_16x16x16bf16_1k(__builtin_bit_cast(s16x4, pp), __builtin_bit_cast(s16x4, bv[k]), acc[k], 0, 0, 0);
  }
  float* mrg = lds + 4 * NSTEP * 128;
  float* my = mrg + wave * PART_F;
#pragma unroll
  for (int u = 0; u < 2; ++u)
#pragma unroll
    for (int dh = 0; dh < 2; ++dh) {
      f32x4 o;
#pragma unroll
      for (int c = 0; c < 4; ++c) o[c] = acc[dh * 4 + c][2 * u] + acc[dh * 4 + c][2 * u + 1];
      *(f32x4*)(my + 16 + (2 * g + u) * D + 64 * dh + 4 * i) = o;
    }
  if (lane < 16 && hf == 0) { my[i >> 1] = mx; my[8 + (i >> 1)] = lsum; }
  __syncthreads();
  {
    const int h = wave, d0 = 2 * lane;
    float mw[NWAVE], M = -INFINITY;
#pragma unroll
    for (int w = 0; w < NWAVE; ++w) { mw[w] = mrg[w * PART_F + h]; M = fmaxf(M, mw[w]); }
    float L = 0.f, o0 = 0.f, o1 = 0.f;
#pragma unroll
    for (int w = 0; w < NWAVE; ++w) {
      const float e = __builtin_amdgcn_exp2f((mw[w] - M) * LOG2E);
      const f32x2 o = *(const f32x2*)(mrg + w * PART_F + 16 + h * D + d0);
      L = fmaf(e, mrg[w * PART_F + 8 + h], L); o0 = fmaf(e, o[0], o0); o1 = fmaf(e, o[1], o1);
    }
    float* pp = part + (size_t)(bh * NSPLIT + split) * PART_F;
    *(f32x2*)(pp + 16 + h * D + d0) = f32x2{o0, o1};
    if (lane == 0) { pp[h] = M; pp[8 + h] = L; }
  }
}

__global__ __launch_bounds__(128) void attn_combine(const float* __restrict__ part, __hip_bfloat16* __restrict__ out) {
  const int row = blockIdx.x, bh = row / REP, h = row % REP, d = threadIdx.x;
  const float* pp = part + (size_t)bh * NSPLIT * PART_F;
  float m[NSPLIT], l[NSPLIT], o[NSPLIT];
#pragma unroll
  for (int s = 0; s < NSPLIT; ++s) { m[s] = pp[s * PART_F + h]; l[s] = pp[s * PART_F + 8 + h]; o[s] = pp[s * PART_F + 16 + h * D + d]; }
  float M = -INFINITY;
#pragma unroll
  for (int s = 0; s < NSPLIT; ++s) M = fmaxf(M, m[s]);
  float L = 0.f, O = 0.f;
#pragma unroll
  for (int s = 0; s < NSPLIT; ++s) { const float e = __builtin_amdgcn_exp2f((m[s] - M) * LOG2E); L = fmaf(e, l[s], L); O = fmaf(e, o[s], O); }
  out[row * D + d] = __float2bfloat16(O / L);
}

extern "C" void kernel_launch(void* const* d_in, const int* in_sizes, int n_in, void* d_out, int out_size, void* d_ws, size_t ws_size,
                              hipStream_t stream) {
  float* part = (float*)d_ws;
  for (int i = 0; i < n_in; ++i) { hipPointerAttribute_t a; hipError_t e = hipPointerGetAttributes(&a, d_in[i]); void* base = nullptr; size_t sz = 0; hipMemGetAddressRange((hipDeviceptr_t*)&base, &sz, (hipDeviceptr_t)d_in[i]);
    fprintf(stderr, "in%d %p err %d type %d managed %d flags %u base %p size %zu\n", i, d_in[i], (int)e, (int)a.type, a.isManaged, a.allocationFlags, base, sz); }
  fprintf(stderr, "out %p ws %p %zu\n", d_out, d_ws, ws_size);
  attn_main<<<NBH * NSPLIT, 512, 0, stream>>>((const unsigned short*)d_in[0], (const int*)d_in[1], (const int*)d_in[2],
                                              (const unsigned short*)d_in[3], (const unsigned short*)d_in[4],
                                              (const unsigned short*)d_in[5], (const unsigned short*)d_in[6], part);
  attn_combine<<<B * HQ, 128, 0, stream>>>(part, (__hip_bfloat16*)d_out);
}
```

```cpp
#include <hip/hip_runtime.h>
#include <hip/hip_bf16.h>
#include <stdint.h>

typedef unsigned u32x4 __attribute__((ext_vector_type(4)));
typedef unsigned u32x2 __attribute__((ext_vector_type(2)));
typedef float    f32x4 __attribute__((ext_vector_type(4)));
typedef float    f32x2 __attribute__((ext_vector_type(2)));
typedef __bf16   bf16x2 __attribute__((ext_vector_type(2)));
typedef __bf16   bf16x8 __attribute__((ext_vector_type(8)));
typedef short    s16x4 __attribute__((ext_vector_type(4)));

#define MAP 1
#define PSLEEP 2
#define PDELAY 16
#define PRETOUCH 1

constexpr int AUX_NT = 2, AUX_SC1 = 16, OOB = 0x40000000;
constexpr int B = 2, HQ = 32, HKV = 4, SKV = 32768, D = 128, G = 128, NG = SKV / G, REP = HQ / HKV;
constexpr int NBH = B * HKV, NWAVE = 8, NSPLIT = 32, NSTEP = NG / NSPLIT;
constexpr int STEP_BYTES = NSPLIT * G * D * 4;
constexpr float LOG2E = 1.4426950408889634f;
constexpr int WS_ML = 4096, WS_O = WS_ML + NBH * NSPLIT * 128, WS_END = WS_O + NBH * NSPLIT * NSPLIT * 256;
constexpr int L_CONST = 0, L_Q = 4 * NSTEP * 128, L_MX = L_Q + REP * D / 2, L_SUM = L_MX + NWAVE * REP, L_MRG = L_SUM + NWAVE * REP, L_END = L_MRG + NWAVE * REP * D;

__device__ __forceinline__ unsigned pk_bf16(float lo, float hi) {
  f32x2 v = {lo, hi};
  return __builtin_bit_cast(unsigned, __builtin_convertvector(v, bf16x2));
}
__device__ __forceinline__ float bf_lo(unsigned u) { return __builtin_bit_cast(float, u << 16); }
__device__ __forceinline__ float bf_hi(unsigned u) { return __builtin_bit_cast(float, u & 0xffff0000u); }
__device__ __forceinline__ float rnd_bf16(float x) { return __builtin_bit_cast(float, pk_bf16(0.f, x)); }
template <int CTRL> __device__ __forceinline__ float dpp(float x) {
  return __builtin_bit_cast(float, __builtin_amdgcn_mov_dpp(__builtin_bit_cast(int, x), CTRL, 0xf, 0xf, true));
}
__device__ __forceinline__ float x16_max(float x) {
  auto s = __builtin_amdgcn_permlane16_swap(__float_as_uint(x), __float_as_uint(x), false, false);
  return fmaxf(__uint_as_float(s[0]), __uint_as_float(s[1]));
}
__device__ __forceinline__ float x16_sum(float x) {
  auto s = __builtin_amdgcn_permlane16_swap(__float_as_uint(x), __float_as_uint(x), false, false);
  return __uint_as_float(s[0]) + __uint_as_float(s[1]);
}
__device__ __forceinline__ float xrow_max(float x) {
  x = x16_max(x);
  auto t = __builtin_amdgcn_permlane32_swap(__float_as_uint(x), __float_as_uint(x), false, false);
  return fmaxf(__uint_as_float(t[0]), __uint_as_float(t[1]));
}
__device__ __forceinline__ float xrow_sum(float x) {
  x = x16_sum(x);
  auto t = __builtin_amdgcn_permlane32_swap(__float_as_uint(x), __float_as_uint(x), false, false);
  return __uint_as_float(t[0]) + __uint_as_float(t[1]);
}
__device__ __forceinline__ unsigned deq2(unsigned c0, unsigned c1, f32x2 z, f32x2 s) {
  f32x2 x = {(float)(int)c0, (float)(int)c1};
  x -= z;
  f32x2 r = {rnd_bf16(x[0]), rnd_bf16(x[1])};
  r *= s;
  return pk_bf16(r[0], r[1]);
}
__device__ __forceinline__ float out_factor(u32x4 ml, float m_own) {
  const float m = __uint_as_float(ml[0]);
  float M = fmaxf(m, dpp<0xB1>(m)); M = fmaxf(M, dpp<0x4E>(M)); M = fmaxf(M, dpp<0x141>(M)); M = fmaxf(M, dpp<0x128>(M)); M = x16_max(M);
  float L = __builtin_amdgcn_exp2f((m - M) * LOG2E) * __uint_as_float(ml[2]);
  L += dpp<0xB1>(L); L += dpp<0x4E>(L); L += dpp<0x141>(L); L += dpp<0x128>(L); L = x16_sum(L);
  return __builtin_amdgcn_exp2f((m_own - M) * LOG2E) / L;
}
#define SB() __builtin_amdgcn_sched_barrier(0)
#define LDS_BARRIER() do { asm volatile("s_waitcnt lgkmcnt(0)" ::: "memory"); __builtin_amdgcn_s_barrier(); asm volatile("" ::: "memory"); } while (0)
#define STAMP(k)
#define STAMP_DECL
#define STAMP_OUT(idx)

__global__ __launch_bounds__(512, 2) void attn_main(const unsigned short* __restrict__ q, const int* __restrict__ qk, const int* __restrict__ qv,
                                                    const unsigned short* __restrict__ ksc, const unsigned short* __restrict__ kzr,
                                                    const unsigned short* __restrict__ vsc, const unsigned short* __restrict__ vzr,
                                                    unsigned* ws, unsigned* __restrict__ out) {
  __shared__ __attribute__((aligned(16))) float lds[L_END];
  STAMP_DECL; STAMP(0);
  const int tid = threadIdx.x, lane = tid & 63, wave = __builtin_amdgcn_readfirstlane(tid >> 6);
  const int wg = blockIdx.x;
  const int bh = MAP ? wg % NBH : wg / NSPLIT, split = MAP ? wg / NBH : wg % NSPLIT;
  const int i = lane & 15, g = lane >> 4, hf = i & 1;
  const size_t tok0 = (size_t)bh * SKV + split * G + wave * 16;
  constexpr int RBYTES = (NSTEP - 1) * STEP_BYTES + 16 * D * 4;
  const int voffK = (4 * (i >> 2) + ((i >> 1) & 1)) * 512 + (2 * g + hf) * 16;
  const int voffV = 4 * g * 512 + i * 16;
  auto rW = __builtin_amdgcn_make_buffer_rsrc((void*)ws, 0, WS_END, 0x00020000);
  const int so_in = WS_O + (bh * NSPLIT + split) * (NSPLIT * 256);

  u32x4 win[8];
#define ISSUE(j, rs, vo, so, str) win[j] = __builtin_amdgcn_raw_buffer_load_b128(rs, (vo) + ((j) >> 2) * 1024, (so) + ((j) & 3) * (str), AUX_NT)
  unsigned epoch, touch = 0;
  {
    auto rK = __builtin_amdgcn_make_buffer_rsrc((void*)(qk + tok0 * D), 0, RBYTES, 0x00020000);
    const int arr = wave >> 1, stp = (wave & 1) * 4 + g;
    const unsigned short* src = arr == 0 ? ksc : arr == 1 ? kzr : arr == 2 ? vsc : vzr;
    const u32x4 st = *(const u32x4*)(src + ((size_t)bh * NG + split + stp * NSPLIT) * 128 + i * 8);
    const unsigned qw = ((const unsigned*)q)[bh * (REP * D / 2) + tid];
    epoch = __hip_atomic_load(ws + bh * 64, __ATOMIC_RELAXED, __HIP_MEMORY_SCOPE_AGENT);
    SB();
#pragma unroll
    for (int j = 0; j < 8; ++j) { ISSUE(j, rK, voffK, 0, 128); SB(); }
    if (PRETOUCH) touch = __builtin_amdgcn_raw_buffer_load_b32(rW, lane < 8 ? lane * 128 : OOB, so_in + wave * 1024, AUX_SC1);
    SB();
    STAMP(1);
    float* dst = lds + L_CONST + (arr * NSTEP + stp) * 128 + i * 8;
    *(f32x4*)dst = f32x4{bf_lo(st[0]), bf_hi(st[0]), bf_lo(st[1]), bf_hi(st[1])};
    *(f32x4*)(dst + 4) = f32x4{bf_lo(st[2]), bf_hi(st[2]), bf_lo(st[3]), bf_hi(st[3])};
    lds[L_Q + tid] = __uint_as_float(qw);
  }
  LDS_BARRIER();
  STAMP(2);
  u32x4 qb[2];
  {
    const u32x2* qp = (const u32x2*)(lds + L_Q + (i >> 1) * (D / 2) + (2 * g + hf) * 2);
    const u32x2 q0 = qp[0], q1 = qp[8], q2 = qp[16], q3 = qp[24];
    qb[0] = u32x4{q0[0], q0[1], q1[0], q1[1]}; qb[1] = u32x4{q2[0], q2[1], q3[0], q3[1]};
  }
  float s[16];
#pragma unroll
  for (int T = 0; T < 16; ++T) s[T] = 0.f;
#pragma unroll 1
  for (int it = 0; it < NSTEP; ++it) {
    const bool last = it == NSTEP - 1;
    auto rP = __builtin_amdgcn_make_buffer_rsrc((void*)((last ? qv : qk) + tok0 * D), 0, RBYTES, 0x00020000);
    const int pvo = last ? voffV : voffK, pso = last ? 0 : (it + 1) * STEP_BYTES, pstr = last ? 256 : 128;
    const float* kc = lds + L_CONST + it * 128 + (2 * g + hf) * 4;
#pragma unroll
    for (int T = 0; T < 14; ++T) s[T] = s[T + 2];
    f32x4 ks[4], kz[4];
#pragma unroll
    for (int p = 0; p < 4; ++p) { ks[p] = *(const f32x4*)(kc + p * 32); kz[p] = *(const f32x4*)(kc + NSTEP * 128 + p * 32); }
    SB();
#pragma unroll
    for (int u = 0; u < 2; ++u) {
      f32x4 d = {0.f, 0.f, 0.f, 0.f};
#pragma unroll
      for (int m = 0; m < 2; ++m) {
        u32x4 a;
#pragma unroll
        for (int e = 0; e < 2; ++e) {
          const int p = 2 * m + e, j = 4 * u + p;
          const u32x4 x = win[j];
          a[2 * e + 0] = deq2(x[0], x[1], f32x2{kz[p][0], kz[p][1]}, f32x2{ks[p][0], ks[p][1]});
          a[2 * e + 1] = deq2(x[2], x[3], f32x2{kz[p][2], kz[p][3]}, f32x2{ks[p][2], ks[p][3]});
          SB();
          ISSUE(j, rP, pvo, pso, pstr);
          SB();
        }
        d = __builtin_amdgcn_mfma_f32_16x16x32_bf16(__builtin_bit_cast(bf16x8, a), __builtin_bit_cast(bf16x8, qb[m]), d, 0, 0, 0);
      }
      const float mine = hf ? d[3] : d[0], send = hf ? d[1] : d[2];
      const float dot = mine + dpp<0xB1>(send);
      s[14 + u] = rnd_bf16(rnd_bf16(dot) * 0.088388347648318440f);
    }
    STAMP(8 + it);
    SB(); __builtin_amdgcn_s_barrier(); SB();
  }
  STAMP(3);
  asm volatile("" :: "v"(touch));
  float mx = s[0];
#pragma unroll
  for (int T = 1; T < 16; ++T) mx = fmaxf(mx, s[T]);
  mx = fmaxf(mx, dpp<0xB1>(mx));
  mx = xrow_max(mx);
  if (lane < 16 && hf == 0) lds[L_MX + (i >> 1) * NWAVE + wave] = mx;
  LDS_BARRIER();
  {
    const f32x4 m0 = *(const f32x4*)(lds + L_MX + (i >> 1) * NWAVE), m1 = *(const f32x4*)(lds + L_MX + (i >> 1) * NWAVE + 4);
    mx = fmaxf(fmaxf(fmaxf(m0[0], m0[1]), fmaxf(m0[2], m0[3])), fmaxf(fmaxf(m1[0], m1[1]), fmaxf(m1[2], m1[3])));
  }
  float lsum = 0.f;
  unsigned pa[16];
#pragma unroll
  for (int T = 0; T < 16; ++T) {
    const float p = __builtin_amdgcn_exp2f((s[T] - mx) * LOG2E);
    lsum += p;
    pa[T] = pk_bf16(hf ? 0.f : p, hf ? p : 0.f);
  }
  lsum += dpp<0xB1>(lsum);
  lsum = xrow_sum(lsum);
  if (lane < 16 && hf == 0) lds[L_SUM + (i >> 1) * NWAVE + wave] = lsum;
  LDS_BARRIER();
  const unsigned tagA = (epoch + 1u) * 0x9E3779B1u, tagB = ~tagA;
  {
    const f32x4 l0 = *(const f32x4*)(lds + L_SUM + (i >> 1) * NWAVE), l1 = *(const f32x4*)(lds + L_SUM + (i >> 1) * NWAVE + 4);
    const float L = ((l0[0] + l0[1]) + (l0[2] + l0[3])) + ((l1[0] + l1[1]) + (l1[2] + l1[3]));
    __builtin_amdgcn_raw_buffer_store_b128(u32x4{__float_as_uint(mx), tagA, __float_as_uint(L), tagB}, rW,
                                           (tid < 16 && hf == 0) ? (i >> 1) * 16 : OOB, WS_ML + (bh * NSPLIT + split) * 128, AUX_SC1);
  }
  const float m_own = __int_as_float(__builtin_amdgcn_readlane(__float_as_int(mx), 2 * wave));
  f32x4 acc[8];
#pragma unroll
  for (int k = 0; k < 8; ++k) acc[k] = f32x4{0.f, 0.f, 0.f, 0.f};
  const int so_ml = WS_ML + bh * (NSPLIT * 128) + wave * 16, vo_ml = (lane & 31) * 128;
  u32x4 ml = {0u, 0u, 0u, 0u};
  float fac = 0.f;
  bool bad = true;
  {
    auto rV = __builtin_amdgcn_make_buffer_rsrc((void*)(qv + tok0 * D), 0, RBYTES, 0x00020000);
#pragma unroll 1
    for (int it = 0; it < NSTEP; ++it) {
      const float* vc = lds + L_CONST + (2 * NSTEP + it) * 128 + wave * 16 + 4 * g;
      const int pso = (it + 1) * STEP_BYTES;
      const int pvo = it == NSTEP - 1 ? OOB : voffV;
      const f32x4 vs = *(const f32x4*)(vc), vz = *(const f32x4*)(vc + NSTEP * 128);
      SB();
      fac = out_factor(ml, m_own);
      bad = ((ml[1] ^ tagA) | (ml[3] ^ tagB)) != 0u;
      SB();
      ml = __builtin_amdgcn_raw_buffer_load_b128(rW, it == NSTEP - 2 ? vo_ml : OOB, so_ml, AUX_SC1);
      SB();
      u32x2 bv[8];
#pragma unroll
      for (int u = 0; u < 2; ++u) {
        const int j0 = 4 * u;
#pragma unroll
        for (int dh = 0; dh < 2; ++dh) {
          const u32x4 x0 = win[j0 + dh], x1 = win[j0 + 2 + dh];
#pragma unroll
          for (int c = 0; c < 4; ++c) bv[dh * 4 + c][u] = deq2(x0[c], x1[c], f32x2{vz[2 * u], vz[2 * u + 1]}, f32x2{vs[2 * u], vs[2 * u + 1]});
        }
        SB();
#pragma unroll
        for (int e = 0; e < 4; ++e) { ISSUE(j0 + e, rV, pvo, pso, 256); SB(); }
      }
      const u32x2 pp = {pa[0], pa[1]};
#pragma unroll
      for (int k = 0; k < 8; ++k)
        acc[k] = __builtin_amdgcn_mfma_f32_16x16x16bf16_1k(__builtin_bit_cast(s16x4, pp), __builtin_bit_cast(s16x4, bv[k]), acc[k], 0, 0, 0);
      STAMP(16 + it);
      if (it != NSTEP - 1) { SB(); __builtin_amdgcn_s_barrier(); SB(); }
#pragma unroll
      for (int T = 0; T < 14; ++T) pa[T] = pa[T + 2];
    }
  }
  STAMP(4);
  float* my = lds + L_MRG + wave * (REP * D);
#pragma unroll
  for (int u = 0; u < 2; ++u)
#pragma unroll
    for (int dh = 0; dh < 2; ++dh) {
      f32x4 o;
#pragma unroll
      for (int c = 0; c < 4; ++c) o[c] = acc[dh * 4 + c][2 * u] + acc[dh * 4 + c][2 * u + 1];
      *(f32x4*)(my + (2 * g + u) * D + 64 * dh + 4 * i) = o;
    }
  if (__builtin_expect(__builtin_amdgcn_ballot_w64(bad) != 0ull, 0)) {
#pragma unroll 1
    for (unsigned spins = 0; spins < (1u << 20); ++spins) {
      asm volatile("" ::: "memory");
      ml = __builtin_amdgcn_raw_buffer_load_b128(rW, vo_ml, so_ml, AUX_SC1);
      if (__builtin_amdgcn_ballot_w64(((ml[1] ^ tagA) | (ml[3] ^ tagB)) != 0u) == 0ull) break;
      __builtin_amdgcn_s_sleep(8);
    }
    fac = out_factor(ml, m_own);
  }
  LDS_BARRIER();
  {
    f32x2 o = {0.f, 0.f};
#pragma unroll
    for (int w = 0; w < NWAVE; ++w) o += *(const f32x2*)(lds + L_MRG + w * (REP * D) + wave * D + 2 * lane);
    o *= fac;
    __builtin_amdgcn_raw_buffer_store_b128(u32x4{__float_as_uint(o[0]), tagA, __float_as_uint(o[1]), tagB}, rW,
                                           g * (NSPLIT * 256) + i * 16, WS_O + (bh * NSPLIT + wave * 4) * (NSPLIT * 256) + split * 256, AUX_SC1);
  }
  STAMP(5);
  if (wave == 0) {
    u32x4 a[8];
    __builtin_amdgcn_s_sleep(PDELAY);
#pragma unroll 1
    for (unsigned spins = 0; spins < (1u << 20); ++spins) {
      asm volatile("" ::: "memory");
#pragma unroll
      for (int k = 0; k < 8; ++k) a[k] = __builtin_amdgcn_raw_buffer_load_b128(rW, lane * 16 + (k & 3) * 1024, so_in + (k >> 2) * 4096, AUX_SC1);
      unsigned x = 0;
#pragma unroll
      for (int k = 0; k < 8; ++k) x |= (a[k][1] ^ tagA) | (a[k][3] ^ tagB);
      if (__builtin_amdgcn_ballot_w64(x != 0u) == 0ull) break;
      __builtin_amdgcn_s_sleep(PSLEEP);
    }
    STAMP(6);
    float r0 = 0.f, r1 = 0.f;
#pragma unroll
    for (int k = 0; k < 8; ++k) { r0 += __uint_as_float(a[k][0]); r1 += __uint_as_float(a[k][2]); }
    r0 = xrow_sum(r0); r1 = xrow_sum(r1);
    if (lane < 16) out[(bh * NSPLIT + split) * 16 + lane] = pk_bf16(r0, r1);
    if (lane == 0 && split == 0) __hip_atomic_store(ws + bh * 64, epoch + 1u, __ATOMIC_RELAXED, __HIP_MEMORY_SCOPE_AGENT);
    STAMP(7);
  }
  STAMP_OUT(wg * 8 + wave);
}

extern "C" void kernel_launch(void* const* d_in, const int* in_sizes, int n_in, void* d_out, int out_size, void* d_ws, size_t ws_size,
                              hipStream_t stream) {
  attn_main<<<NBH * NSPLIT, 512, 0, stream>>>((const unsigned short*)d_in[0], (const int*)d_in[1], (const int*)d_in[2],
                                              (const unsigned short*)d_in[3], (const unsigned short*)d_in[4],
                                              (const unsigned short*)d_in[5], (const unsigned short*)d_in[6], (unsigned*)d_ws, (unsigned*)d_out);
}
```

```cpp
#include <hip/hip_runtime.h>
#include <hip/hip_bf16.h>
#include <stdint.h>

typedef unsigned u32x4 __attribute__((ext_vector_type(4)));
typedef unsigned u32x2 __attribute__((ext_vector_type(2)));
typedef float    f32x4 __attribute__((ext_vector_type(4)));
typedef float    f32x2 __attribute__((ext_vector_type(2)));
typedef __bf16   bf16x2 __attribute__((ext_vector_type(2)));
typedef __bf16   bf16x8 __attribute__((ext_vector_type(8)));
typedef short    s16x4 __attribute__((ext_vector_type(4)));

#define MAP 1
#define PSLEEP 2
#define PDELAY 16
#define PRETOUCH 1

#define ROTK 1
#define ROTW 0
#define KFIRST 0
#define OUTAUX 0
#define OUTTOUCH 0
#define KLATE 0
#define TLBPF 0
#define THR 3
#define THRM 0xff
#define THO 63
#define CODEPF 2
#define TAU 0
#define TA0 250
constexpr int AUX_NT = 2, AUX_SC1 = 16, OOB = 0x40000000;
constexpr int B = 2, HQ = 32, HKV = 4, SKV = 32768, D = 128, G = 128, NG = SKV / G, REP = HQ / HKV;
constexpr int NBH = B * HKV, NWAVE = 8, NSPLIT = 32, NSTEP = NG / NSPLIT;
constexpr int STEP_BYTES = NSPLIT * G * D * 4;
constexpr float LOG2E = 1.4426950408889634f;
constexpr int WS_ML = 4096, WS_O = WS_ML + NBH * NSPLIT * 128, WS_END = WS_O + NBH * NSPLIT * NSPLIT * 256;
constexpr int L_CONST = 0, L_Q = 4 * NSTEP * 128, L_MX = L_Q + REP * D / 2, L_SUM = L_MX + NWAVE * REP, L_MRG = L_SUM + NWAVE * REP, L_END = L_MRG + NWAVE * REP * D;

__device__ __forceinline__ unsigned pk_bf16(float lo, float hi) {
  f32x2 v = {lo, hi};
  return __builtin_bit_cast(unsigned, __builtin_convertvector(v, bf16x2));
}
__device__ __forceinline__ float bf_lo(unsigned u) { return __builtin_bit_cast(float, u << 16); }
__device__ __forceinline__ float bf_hi(unsigned u) { return __builtin_bit_cast(float, u & 0xffff0000u); }
__device__ __forceinline__ float rnd_bf16(float x) { return __builtin_bit_cast(float, pk_bf16(0.f, x)); }
template <int CTRL> __device__ __forceinline__ float dpp(float x) {
  return __builtin_bit_cast(float, __builtin_amdgcn_mov_dpp(__builtin_bit_cast(int, x), CTRL, 0xf, 0xf, true));
}
__device__ __forceinline__ float x16_max(float x) {
  auto s = __builtin_amdgcn_permlane16_swap(__float_as_uint(x), __float_as_uint(x), false, false);
  return fmaxf(__uint_as_float(s[0]), __uint_as_float(s[1]));
}
__device__ __forceinline__ float x16_sum(float x) {
  auto s = __builtin_amdgcn_permlane16_swap(__float_as_uint(x), __float_as_uint(x), false, false);
  return __uint_as_float(s[0]) + __uint_as_float(s[1]);
}
__device__ __forceinline__ float xrow_max(float x) {
  x = x16_max(x);
  auto t = __builtin_amdgcn_permlane32_swap(__float_as_uint(x), __float_as_uint(x), false, false);
  return fmaxf(__uint_as_float(t[0]), __uint_as_float(t[1]));
}
__device__ __forceinline__ float xrow_sum(float x) {
  x = x16_sum(x);
  auto t = __builtin_amdgcn_permlane32_swap(__float_as_uint(x), __float_as_uint(x), false, false);
  return __uint_as_float(t[0]) + __uint_as_float(t[1]);
}
__device__ __forceinline__ unsigned deq2(unsigned c0, unsigned c1, f32x2 z, f32x2 s) {
  f32x2 x = {(float)(int)c0, (float)(int)c1};
  x -= z;
  f32x2 r = {rnd_bf16(x[0]), rnd_bf16(x[1])};
  r *= s;
  return pk_bf16(r[0], r[1]);
}
__device__ __forceinline__ float out_factor(u32x4 ml, float m_own) {
  const float m = __uint_as_float(ml[0]);
  float M = fmaxf(m, dpp<0xB1>(m)); M = fmaxf(M, dpp<0x4E>(M)); M = fmaxf(M, dpp<0x141>(M)); M = fmaxf(M, dpp<0x128>(M)); M = x16_max(M);
  float L = __builtin_amdgcn_exp2f((m - M) * LOG2E) * __uint_as_float(ml[2]);
  L += dpp<0xB1>(L); L += dpp<0x4E>(L); L += dpp<0x141>(L); L += dpp<0x128>(L); L = x16_sum(L);
  return __builtin_amdgcn_exp2f((m_own - M) * LOG2E) / L;
}
#define SB() __builtin_amdgcn_sched_barrier(0)
#define LDS_BARRIER() do { asm volatile("s_waitcnt lgkmcnt(0)" ::: "memory"); __builtin_amdgcn_s_barrier(); asm volatile("" ::: "memory"); } while (0)
#define STAMP(k)
#define STAMP_DECL
#define STAMP_OUT(idx)

__global__ __launch_bounds__(512, 2) void attn_main(const unsigned short* __restrict__ q, const int* __restrict__ qk, const int* __restrict__ qv,
                                                    const unsigned short* __restrict__ ksc, const unsigned short* __restrict__ kzr,
                                                    const unsigned short* __restrict__ vsc, const unsigned short* __restrict__ vzr,
                                                    unsigned* ws, unsigned* __restrict__ out) {
  __shared__ __attribute__((aligned(16))) float lds[L_END];
  STAMP_DECL; STAMP(0);
  const unsigned t_start = TAU ? (unsigned)__builtin_amdgcn_s_memrealtime() : 0u;
  int fast = (int)(__builtin_amdgcn_s_getreg(20 | (3 << 11)) & 1) - 1;
#define PACE(n) do { if (TAU) { const unsigned now_ = (unsigned)__builtin_amdgcn_s_memrealtime(); fast = (int)(now_ - t_start) - (TA0 + (n) * TAU); SB(); } } while (0)
  const int tid = threadIdx.x, lane = tid & 63, wave = __builtin_amdgcn_readfirstlane(tid >> 6);
  const int wg = blockIdx.x;
  const int bh = MAP == 1 ? wg % NBH : MAP == 0 ? wg / NSPLIT : MAP == 2 ? (wg & 6) | (wg >> 7) : MAP == 3 ? (wg & 6) | ((wg >> 3) & 1) : (wg & 4) | (wg >> 6);
  const int split = MAP == 1 ? wg / NBH : MAP == 0 ? wg % NSPLIT : MAP == 2 ? (wg & 1) * 16 + ((wg >> 3) & 15) : MAP == 3 ? (wg & 1) + 2 * (wg >> 4) : (wg & 3) * 8 + ((wg >> 3) & 7);
  const int i = lane & 15, g = lane >> 4, hf = i & 1;
  const int rot = bh * ROTK + split * ROTW;
#define SOFF(st) ((((st) + rot) & 7) * STEP_BYTES)
  const size_t tok0 = (size_t)bh * SKV + split * G + wave * 16;
  constexpr int RBYTES = (NSTEP - 1) * STEP_BYTES + 16 * D * 4;
  const int voffK = (4 * (i >> 2) + ((i >> 1) & 1)) * 512 + (2 * g + hf) * 16;
  const int voffV = 4 * g * 512 + i * 16;
  auto rW = __builtin_amdgcn_make_buffer_rsrc((void*)ws, 0, WS_END, 0x00020000);
  const int so_in = WS_O + (bh * NSPLIT + split) * (NSPLIT * 256);

  u32x4 win[8];
#define ISSUE(j, rs, vo, so, str) win[j] = __builtin_amdgcn_raw_buffer_load_b128(rs, (vo) + ((j) >> 2) * 1024, (so) + ((j) & 3) * (str), AUX_NT)
#define THROTTLE(j) do { if (THR) asm volatile("s_bitcmp0_b32 %0, 31\n\ts_cbranch_scc1 1f\n\ts_waitcnt vmcnt(%c1)\n1:\n\ts_waitcnt vmcnt(%c2)" :: "s"(fast), "i"(((THRM >> (j)) & 1) ? THR : THR + 1), "i"(THO) : "scc"); SB(); } while (0)
  unsigned epoch, touch = 0, touch2 = 0, touch3 = 0;
  {
    auto rK = __builtin_amdgcn_make_buffer_rsrc((void*)(qk + tok0 * D), 0, RBYTES, 0x00020000);
#pragma unroll
    for (int j = 0; j < KFIRST; ++j) { ISSUE(j, rK, voffK, SOFF(0), 128); SB(); }
    const int arr = wave >> 1, stp = (wave & 1) * 4 + g;
    const unsigned short* src = arr == 0 ? ksc : arr == 1 ? kzr : arr == 2 ? vsc : vzr;
    const u32x4 st = *(const u32x4*)(src + ((size_t)bh * NG + split + ((stp + rot) & 7) * NSPLIT) * 128 + i * 8);
    const unsigned qw = ((const unsigned*)q)[bh * (REP * D / 2) + tid];
    if (CODEPF) {
      unsigned long long pc; unsigned len;
      asm volatile("s_getpc_b64 %0\n.Lattn_head_mark:\n\ts_mov_b32 %1, .Lattn_tail_mark-.Lattn_head_mark" : "=s"(pc), "=s"(len));
      auto rC = __builtin_amdgcn_make_buffer_rsrc((void*)pc, 0, len, 0x00020000);
      touch3 = __builtin_amdgcn_raw_buffer_load_b32(rC, CODEPF == 1 ? (wave == 7 ? lane * 128 : OOB) : (lane < 16 ? (wave * 16 + lane) * 64 : OOB), 0, 0);
    }
    epoch = __hip_atomic_load(ws + bh * 64, __ATOMIC_RELAXED, __HIP_MEMORY_SCOPE_AGENT);
    SB();
#pragma unroll
    for (int j = KFIRST; j < 8 - KLATE; ++j) { ISSUE(j, rK, voffK, SOFF(0), 128); SB(); }
    if (PRETOUCH == 3) __builtin_amdgcn_raw_buffer_store_b128(u32x4{0u, 0u, 0u, 0u}, rW, g * (NSPLIT * 256) + i * 16, WS_O + (bh * NSPLIT + wave * 4) * (NSPLIT * 256) + split * 256, AUX_SC1);
    SB();
    STAMP(1);
    float* dst = lds + L_CONST + (arr * NSTEP + stp) * 128 + i * 8;
    *(f32x4*)dst = f32x4{bf_lo(st[0]), bf_hi(st[0]), bf_lo(st[1]), bf_hi(st[1])};
    *(f32x4*)(dst + 4) = f32x4{bf_lo(st[2]), bf_hi(st[2]), bf_lo(st[3]), bf_hi(st[3])};
    lds[L_Q + tid] = __uint_as_float(qw);
    LDS_BARRIER();
    SB();
#pragma unroll
    for (int j = 8 - KLATE; j < 8; ++j) { ISSUE(j, rK, voffK, SOFF(0), 128); SB(); }
    if (PRETOUCH == 1) touch = __builtin_amdgcn_raw_buffer_load_b32(rW, lane < 8 ? lane * 128 : OOB, so_in + wave * 1024, AUX_SC1);
    if (TLBPF == 1) { const int* pp = ((lane & 1) ? qv : qk) + ((size_t)bh * SKV + split * G) * D + (size_t)wave * (STEP_BYTES / 4); if (lane < 2) touch2 = __builtin_nontemporal_load(pp); }
    if (TLBPF == 2) { const int* pp = ((lane & 8) ? qv : qk) + ((size_t)bh * SKV + split * G) * D + (size_t)(lane & 7) * (STEP_BYTES / 4); if (lane < 16 && wave == 7) touch2 = __builtin_nontemporal_load(pp); }
    SB();
  }
  STAMP(2);
  u32x4 qb[2];
  {
    const u32x2* qp = (const u32x2*)(lds + L_Q + (i >> 1) * (D / 2) + (2 * g + hf) * 2);
    const u32x2 q0 = qp[0], q1 = qp[8], q2 = qp[16], q3 = qp[24];
    qb[0] = u32x4{q0[0], q0[1], q1[0], q1[1]}; qb[1] = u32x4{q2[0], q2[1], q3[0], q3[1]};
  }
  float s[16];
#pragma unroll
  for (int T = 0; T < 16; ++T) s[T] = 0.f;
#pragma unroll 1
  for (int it = 0; it < NSTEP; ++it) {
    PACE(it);
    const bool last = it == NSTEP - 1;
    auto rP = __builtin_amdgcn_make_buffer_rsrc((void*)((last ? qv : qk) + tok0 * D), 0, RBYTES, 0x00020000);
    const int pvo = last ? voffV : voffK, pso = SOFF(it + 1), pstr = last ? 256 : 128;
    const float* kc = lds + L_CONST + it * 128 + (2 * g + hf) * 4;
#pragma unroll
    for (int T = 0; T < 14; ++T) s[T] = s[T + 2];
    f32x4 ks[4], kz[4];
#pragma unroll
    for (int p = 0; p < 4; ++p) { ks[p] = *(const f32x4*)(kc + p * 32); kz[p] = *(const f32x4*)(kc + NSTEP * 128 + p * 32); }
    SB();
#pragma unroll
    for (int u = 0; u < 2; ++u) {
      f32x4 d = {0.f, 0.f, 0.f, 0.f};
#pragma unroll
      for (int m = 0; m < 2; ++m) {
        u32x4 a;
#pragma unroll
        for (int e = 0; e < 2; ++e) {
          const int p = 2 * m + e, j = 4 * u + p;
          const u32x4 x = win[j];
          a[2 * e + 0] = deq2(x[0], x[1], f32x2{kz[p][0], kz[p][1]}, f32x2{ks[p][0], ks[p][1]});
          a[2 * e + 1] = deq2(x[2], x[3], f32x2{kz[p][2], kz[p][3]}, f32x2{ks[p][2], ks[p][3]});
          SB();
          THROTTLE(j); ISSUE(j, rP, pvo, pso, pstr);
          SB();
        }
        d = __builtin_amdgcn_mfma_f32_16x16x32_bf16(__builtin_bit_cast(bf16x8, a), __builtin_bit_cast(bf16x8, qb[m]), d, 0, 0, 0);
      }
      const float mine = hf ? d[3] : d[0], send = hf ? d[1] : d[2];
      const float dot = mine + dpp<0xB1>(send);
      s[14 + u] = rnd_bf16(rnd_bf16(dot) * 0.088388347648318440f);
    }
    STAMP(8 + it);
    SB(); __builtin_amdgcn_s_barrier(); SB();
  }
  STAMP(3);
  asm volatile("" :: "v"(touch), "v"(touch2), "v"(touch3));
  float mx = s[0];
#pragma unroll
  for (int T = 1; T < 16; ++T) mx = fmaxf(mx, s[T]);
  mx = fmaxf(mx, dpp<0xB1>(mx));
  mx = xrow_max(mx);
  if (lane < 16 && hf == 0) lds[L_MX + (i >> 1) * NWAVE + wave] = mx;
  LDS_BARRIER();
  {
    const f32x4 m0 = *(const f32x4*)(lds + L_MX + (i >> 1) * NWAVE), m1 = *(const f32x4*)(lds + L_MX + (i >> 1) * NWAVE + 4);
    mx = fmaxf(fmaxf(fmaxf(m0[0], m0[1]), fmaxf(m0[2], m0[3])), fmaxf(fmaxf(m1[0], m1[1]), fmaxf(m1[2], m1[3])));
  }
  float lsum = 0.f;
  unsigned pa[16];
#pragma unroll
  for (int T = 0; T < 16; ++T) {
    const float p = __builtin_amdgcn_exp2f((s[T] - mx) * LOG2E);
    lsum += p;
    pa[T] = pk_bf16(hf ? 0.f : p, hf ? p : 0.f);
  }
  lsum += dpp<0xB1>(lsum);
  lsum = xrow_sum(lsum);
  if (lane < 16 && hf == 0) lds[L_SUM + (i >> 1) * NWAVE + wave] = lsum;
  LDS_BARRIER();
  const unsigned tagA = (epoch + 1u) * 0x9E3779B1u, tagB = ~tagA;
  {
    const f32x4 l0 = *(const f32x4*)(lds + L_SUM + (i >> 1) * NWAVE), l1 = *(const f32x4*)(lds + L_SUM + (i >> 1) * NWAVE + 4);
    const float L = ((l0[0] + l0[1]) + (l0[2] + l0[3])) + ((l1[0] + l1[1]) + (l1[2] + l1[3]));
    __builtin_amdgcn_raw_buffer_store_b128(u32x4{__float_as_uint(mx), tagA, __float_as_uint(L), tagB}, rW,
                                           (tid < 16 && hf == 0) ? (i >> 1) * 16 : OOB, WS_ML + (bh * NSPLIT + split) * 128, AUX_SC1);
  }
  if (PRETOUCH == 2) __builtin_amdgcn_raw_buffer_store_b128(u32x4{0u, 0u, 0u, 0u}, rW, g * (NSPLIT * 256) + i * 16, WS_O + (bh * NSPLIT + wave * 4) * (NSPLIT * 256) + split * 256, AUX_SC1);
  auto rO = __builtin_amdgcn_make_buffer_rsrc((void*)out, 0, B * HQ * D * 2, 0x00020000);
  if (OUTTOUCH) touch = __builtin_amdgcn_raw_buffer_load_b32(rO, tid == 0 ? 0 : OOB, (bh * NSPLIT + split) * 64, 0);
  const float m_own = __int_as_float(__builtin_amdgcn_readlane(__float_as_int(mx), 2 * wave));
  f32x4 acc[8];
#pragma unroll
  for (int k = 0; k < 8; ++k) acc[k] = f32x4{0.f, 0.f, 0.f, 0.f};
  const int so_ml = WS_ML + bh * (NSPLIT * 128) + wave * 16, vo_ml = (lane & 31) * 128;
  u32x4 ml = {0u, 0u, 0u, 0u};
  float fac = 0.f;
  bool bad = true;
  {
    auto rV = __builtin_amdgcn_make_buffer_rsrc((void*)(qv + tok0 * D), 0, RBYTES, 0x00020000);
#pragma unroll 1
    for (int it = 0; it < NSTEP; ++it) {
      PACE(8 + it);
      const float* vc = lds + L_CONST + (2 * NSTEP + it) * 128 + wave * 16 + 4 * g;
      const int pso = SOFF(it + 1);
      const int pvo = it == NSTEP - 1 ? OOB : voffV;
      const f32x4 vs = *(const f32x4*)(vc), vz = *(const f32x4*)(vc + NSTEP * 128);
      SB();
      fac = out_factor(ml, m_own);
      bad = ((ml[1] ^ tagA) | (ml[3] ^ tagB)) != 0u;
      SB();
      ml = __builtin_amdgcn_raw_buffer_load_b128(rW, it == NSTEP - 2 ? vo_ml : OOB, so_ml, AUX_SC1);
      SB();
      u32x2 bv[8];
#pragma unroll
      for (int u = 0; u < 2; ++u) {
        const int j0 = 4 * u;
#pragma unroll
        for (int dh = 0; dh < 2; ++dh) {
          const u32x4 x0 = win[j0 + dh], x1 = win[j0 + 2 + dh];
#pragma unroll
          for (int c = 0; c < 4; ++c) bv[dh * 4 + c][u] = deq2(x0[c], x1[c], f32x2{vz[2 * u], vz[2 * u + 1]}, f32x2{vs[2 * u], vs[2 * u + 1]});
        }
        SB();
#pragma unroll
        for (int e = 0; e < 4; ++e) { THROTTLE(j0 + e); ISSUE(j0 + e, rV, pvo, pso, 256); SB(); }
      }
      const u32x2 pp = {pa[0], pa[1]};
#pragma unroll
      for (int k = 0; k < 8; ++k)
        acc[k] = __builtin_amdgcn_mfma_f32_16x16x16bf16_1k(__builtin_bit_cast(s16x4, pp), __builtin_bit_cast(s16x4, bv[k]), acc[k], 0, 0, 0);
      STAMP(16 + it);
      if (it != NSTEP - 1) { SB(); __builtin_amdgcn_s_barrier(); SB(); }
#pragma unroll
      for (int T = 0; T < 14; ++T) pa[T] = pa[T + 2];
    }
  }
  STAMP(4);
  if (OUTTOUCH) asm volatile("" :: "v"(touch));
  float* my = lds + L_MRG + wave * (REP * D);
#pragma unroll
  for (int u = 0; u < 2; ++u)
#pragma unroll
    for (int dh = 0; dh < 2; ++dh) {
      f32x4 o;
#pragma unroll
      for (int c = 0; c < 4; ++c) o[c] = acc[dh * 4 + c][2 * u] + acc[dh * 4 + c][2 * u + 1];
      *(f32x4*)(my + (2 * g + u) * D + 64 * dh + 4 * i) = o;
    }
  if (__builtin_expect(__builtin_amdgcn_ballot_w64(bad) != 0ull, 0)) {
#pragma unroll 1
    for (unsigned spins = 0; spins < (1u << 20); ++spins) {
      asm volatile("" ::: "memory");
      ml = __builtin_amdgcn_raw_buffer_load_b128(rW, vo_ml, so_ml, AUX_SC1);
      if (__builtin_amdgcn_ballot_w64(((ml[1] ^ tagA) | (ml[3] ^ tagB)) != 0u) == 0ull) break;
      __builtin_amdgcn_s_sleep(8);
    }
    fac = out_factor(ml, m_own);
  }
  LDS_BARRIER();
  {
    f32x2 o = {0.f, 0.f};
#pragma unroll
    for (int w = 0; w < NWAVE; ++w) o += *(const f32x2*)(lds + L_MRG + w * (REP * D) + wave * D + 2 * lane);
    o *= fac;
    __builtin_amdgcn_raw_buffer_store_b128(u32x4{__float_as_uint(o[0]), tagA, __float_as_uint(o[1]), tagB}, rW,
                                           g * (NSPLIT * 256) + i * 16, WS_O + (bh * NSPLIT + wave * 4) * (NSPLIT * 256) + split * 256, AUX_SC1);
  }
  STAMP(5);
  if (wave == 0) {
    u32x4 a[8];
    __builtin_amdgcn_s_sleep(PDELAY);
#pragma unroll 1
    for (unsigned spins = 0; spins < (1u << 20); ++spins) {
      asm volatile("" ::: "memory");
#pragma unroll
      for (int k = 0; k < 8; ++k) a[k] = __builtin_amdgcn_raw_buffer_load_b128(rW, lane * 16 + (k & 3) * 1024, so_in + (k >> 2) * 4096, AUX_SC1);
      unsigned x = 0;
#pragma unroll
      for (int k = 0; k < 8; ++k) x |= (a[k][1] ^ tagA) | (a[k][3] ^ tagB);
      if (__builtin_amdgcn_ballot_w64(x != 0u) == 0ull) break;
      __builtin_amdgcn_s_sleep(PSLEEP);
    }
    STAMP(6);
    float r0 = 0.f, r1 = 0.f;
#pragma unroll
    for (int k = 0; k < 8; ++k) { r0 += __uint_as_float(a[k][0]); r1 += __uint_as_float(a[k][2]); }
    r0 = xrow_sum(r0); r1 = xrow_sum(r1);
    __builtin_amdgcn_raw_buffer_store_b32(pk_bf16(r0, r1), rO, lane < 16 ? lane * 4 : OOB, (bh * NSPLIT + split) * 64, OUTAUX);
    if (lane == 0 && split == 0) __hip_atomic_store(ws + bh * 64, epoch + 1u, __ATOMIC_RELAXED, __HIP_MEMORY_SCOPE_AGENT);
    if (CODEPF) asm volatile(".Lattn_tail_mark:");
    STAMP(7);
  }
  STAMP_OUT(wg * 8 + wave);
}

extern "C" void kernel_launch(void* const* d_in, const int* in_sizes, int n_in, void* d_out, int out_size, void* d_ws, size_t ws_size,
                              hipStream_t stream) {
  attn_main<<<NBH * NSPLIT, 512, 0, stream>>>((const unsigned short*)d_in[0], (const int*)d_in[1], (const int*)d_in[2],
                                              (const unsigned short*)d_in[3], (const unsigned short*)d_in[4],
                                              (const unsigned short*)d_in[5], (const unsigned short*)d_in[6], (unsigned*)d_ws, (unsigned*)d_out);
}
```

```cpp
#include <hip/hip_runtime.h>
#include <hip/hip_bf16.h>
#include <stdint.h>

typedef unsigned u32x4 __attribute__((ext_vector_type(4)));
typedef unsigned u32x2 __attribute__((ext_vector_type(2)));
typedef float    f32x4 __attribute__((ext_vector_type(4)));
typedef float    f32x2 __attribute__((ext_vector_type(2)));
typedef __bf16   bf16x2 __attribute__((ext_vector_type(2)));
typedef __bf16   bf16x8 __attribute__((ext_vector_type(8)));
typedef short    s16x4 __attribute__((ext_vector_type(4)));

#define MAP 1
#define ROTK 1
#define THR 3
#define THRV 3
#define THRV2 0
#define MLT 1
#define MLSTEP (NSTEP - 2)
#define AMLATE 1
#define CODEPF 0
#define KFIRST 0

constexpr int AUX_NT = 2, AUX_SC1 = 16, OOB = 0x40000000, RSRC = 0x00020000;
constexpr int B = 2, HQ = 32, HKV = 4, SKV = 32768, D = 128, G = 128, NG = SKV / G, REP = HQ / HKV;
constexpr int NBH = B * HKV, NWAVE = 8, NSPLIT = 32, NSTEP = NG / NSPLIT;
constexpr int STEP_BYTES = NSPLIT * G * D * 4;
constexpr float LOG2E = 1.4426950408889634f;
constexpr int WS_ML = 4096, WS_ACC = WS_ML + NBH * NSPLIT * 128, WS_END = WS_ACC + NBH * REP * D * 8;
constexpr int L_CONST = 0, L_Q = 4 * NSTEP * 128, L_MX = L_Q + REP * D / 2, L_SUM = L_MX + NWAVE * REP, L_AMAX = L_SUM + NWAVE * REP,
              L_MRG = L_AMAX + NWAVE, L_END = L_MRG + NWAVE * REP * D;

__device__ __forceinline__ unsigned pk_bf16(float lo, float hi) {
  f32x2 v = {lo, hi};
  return __builtin_bit_cast(unsigned, __builtin_convertvector(v, bf16x2));
}
__device__ __forceinline__ float bf_lo(unsigned u) { return __builtin_bit_cast(float, u << 16); }
__device__ __forceinline__ float bf_hi(unsigned u) { return __builtin_bit_cast(float, u & 0xffff0000u); }
__device__ __forceinline__ float rnd_bf16(float x) { return __builtin_bit_cast(float, pk_bf16(0.f, x)); }
template <int CTRL> __device__ __forceinline__ unsigned dppu(unsigned x) { return (unsigned)__builtin_amdgcn_mov_dpp((int)x, CTRL, 0xf, 0xf, true); }
template <int CTRL> __device__ __forceinline__ float dpp(float x) { return __uint_as_float(dppu<CTRL>(__float_as_uint(x))); }
constexpr int X1 = 0xB1, X2 = 0x4E, X7 = 0x141, X8 = 0x128;
__device__ __forceinline__ float x16_max(float x) {
  auto s = __builtin_amdgcn_permlane16_swap(__float_as_uint(x), __float_as_uint(x), false, false);
  return fmaxf(__uint_as_float(s[0]), __uint_as_float(s[1]));
}
__device__ __forceinline__ float x16_sum(float x) {
  auto s = __builtin_amdgcn_permlane16_swap(__float_as_uint(x), __float_as_uint(x), false, false);
  return __uint_as_float(s[0]) + __uint_as_float(s[1]);
}
__device__ __forceinline__ float xrow_max(float x) {
  x = x16_max(x);
  auto t = __builtin_amdgcn_permlane32_swap(__float_as_uint(x), __float_as_uint(x), false, false);
  return fmaxf(__uint_as_float(t[0]), __uint_as_float(t[1]));
}
__device__ __forceinline__ float xrow_sum(float x) {
  x = x16_sum(x);
  auto t = __builtin_amdgcn_permlane32_swap(__float_as_uint(x), __float_as_uint(x), false, false);
  return __uint_as_float(t[0]) + __uint_as_float(t[1]);
}
__device__ __forceinline__ unsigned umax32(unsigned x) {
  x = max(x, dppu<X1>(x)); x = max(x, dppu<X2>(x)); x = max(x, dppu<X7>(x)); x = max(x, dppu<X8>(x));
  auto s = __builtin_amdgcn_permlane16_swap(x, x, false, false);
  return max(s[0], s[1]);
}
__device__ __forceinline__ unsigned deq2(unsigned c0, unsigned c1, f32x2 z, f32x2 s) {
  f32x2 x = {(float)(int)c0, (float)(int)c1};
  x -= z;
  f32x2 r = {rnd_bf16(x[0]), rnd_bf16(x[1])};
  r *= s;
  return pk_bf16(r[0], r[1]);
}
__device__ __forceinline__ float out_factor(u32x4 ml, float m_own) {
  const float m = __uint_as_float(ml[0]);
  float M = fmaxf(m, dpp<X1>(m)); M = fmaxf(M, dpp<X2>(M)); M = fmaxf(M, dpp<X7>(M)); M = fmaxf(M, dpp<X8>(M)); M = x16_max(M);
  float L = __builtin_amdgcn_exp2f((m - M) * LOG2E) * __uint_as_float(ml[2]);
  L += dpp<X1>(L); L += dpp<X2>(L); L += dpp<X7>(L); L += dpp<X8>(L); L = x16_sum(L);
  return __builtin_amdgcn_exp2f((m_own - M) * LOG2E) / L;
}
#define SB() __builtin_amdgcn_sched_barrier(0)
#define LDS_BARRIER() do { asm volatile("s_waitcnt lgkmcnt(0)" ::: "memory"); __builtin_amdgcn_s_barrier(); asm volatile("" ::: "memory"); } while (0)
#define STAMP(k)
#define STAMP_DECL
#define STAMP_OUT(idx)

__global__ __launch_bounds__(512, 2) void attn_main(const unsigned short* __restrict__ q, const int* __restrict__ qk, const int* __restrict__ qv,
                                                    const unsigned short* __restrict__ ksc, const unsigned short* __restrict__ kzr,
                                                    const unsigned short* __restrict__ vsc, const unsigned short* __restrict__ vzr,
                                                    unsigned* ws, unsigned short* __restrict__ out) {
  __shared__ __attribute__((aligned(16))) float lds[L_END];
  STAMP_DECL; STAMP(0);
  const unsigned tid = threadIdx.x, lane = tid & 63, wave = __builtin_amdgcn_readfirstlane(tid >> 6);
  unsigned warm = 0;
  if (CODEPF) {
    unsigned long long pc; unsigned len;
    asm volatile("s_getpc_b64 %0\n.Lattn_head_mark:\n\ts_mov_b32 %1, .Lattn_tail_mark-.Lattn_head_mark" : "=s"(pc), "=s"(len));
    warm = __builtin_amdgcn_raw_buffer_load_b32(__builtin_amdgcn_make_buffer_rsrc((void*)pc, 0, len, RSRC), lane < 16 ? tid * 16 + lane * 48 : OOB, 0, 0);
    SB();
  }
  const unsigned wg = blockIdx.x;
  const unsigned bh = MAP ? wg % NBH : wg / NSPLIT, split = MAP ? wg / NBH : wg % NSPLIT;
  const unsigned i = lane & 15, g = lane >> 4, hf = i & 1;
  const unsigned rot = bh * ROTK;
#define SOFF(st) ((((st) + rot) & 7) * STEP_BYTES)
  const size_t tile0 = (size_t)((bh * SKV + split * G + wave * 16) * D);
  constexpr int RBYTES = (NSTEP - 1) * STEP_BYTES + 16 * D * 4;
  const int voffK = (4 * (i >> 2) + ((i >> 1) & 1)) * 512 + (2 * g + hf) * 16;
  const int voffV = 4 * g * 512 + i * 16;
  const int odd_xcd = (int)(__builtin_amdgcn_s_getreg(20 | (3 << 11)) & 1);
  auto rW = __builtin_amdgcn_make_buffer_rsrc((void*)ws, 0, WS_END, RSRC);

  u32x4 win[8];
#define ISSUE(j, rs, vo, so, sa) win[j] = __builtin_amdgcn_raw_buffer_load_b128(rs, (vo) + ((j) >> 2) * 1024 + (((j) >> 1) & 1) * 256, (so) + ((j) & 1) * (sa), AUX_NT)
#define THROTTLE(n) do { if (n) { asm volatile("s_cmp_lg_u32 %0, 0\n\ts_cbranch_scc1 1f\n\ts_waitcnt vmcnt(%c1)\n1:" :: "s"(odd_xcd), "i"(n) : "scc"); SB(); } } while (0)
  unsigned epoch;
  {
    auto rK = __builtin_amdgcn_make_buffer_rsrc((void*)(qk + tile0), 0, RBYTES, RSRC);
#pragma unroll
    for (int j = 0; j < KFIRST; ++j) { ISSUE(j, rK, voffK, SOFF(0), 128); SB(); }
    const unsigned arr = wave >> 1, stp = (wave & 1) * 4 + g;
    const unsigned short* src = arr == 0 ? ksc : arr == 1 ? kzr : arr == 2 ? vsc : vzr;
    const u32x4 st = __builtin_amdgcn_raw_buffer_load_b128(__builtin_amdgcn_make_buffer_rsrc((void*)src, 0, NBH * NG * 256, RSRC),
                                                           ((stp + rot) & 7) * (NSPLIT * 256) + i * 16, (bh * NG + split) * 256, 0);
    const unsigned qw = __builtin_amdgcn_raw_buffer_load_b32(__builtin_amdgcn_make_buffer_rsrc((void*)q, 0, B * HQ * D * 2, RSRC), tid * 4, bh * (REP * D * 2), 0);
    epoch = __builtin_amdgcn_raw_buffer_load_b32(rW, 0, bh * 256, AUX_SC1);
    __builtin_amdgcn_raw_buffer_store_b128(u32x4{0u, 0u, 0u, 0u}, rW, tid - 7 * 64 < 16 ? lane * 16 : OOB, WS_ACC + (bh * NSPLIT + split) * 256, AUX_SC1);
    SB();
    asm volatile("s_cmp_lg_u32 %0, 7\n\ts_cbranch_scc1 1f\n\ts_waitcnt vmcnt(0)\n1:" :: "s"(wave) : "scc", "memory");
    SB();
#pragma unroll
    for (int j = KFIRST; j < 8; ++j) { ISSUE(j, rK, voffK, SOFF(0), 128); SB(); }
    STAMP(1);
    float* dst = lds + L_CONST + (arr * NSTEP + stp) * 128 + i * 8;
    *(f32x4*)dst = f32x4{bf_lo(st[0]), bf_hi(st[0]), bf_lo(st[1]), bf_hi(st[1])};
    *(f32x4*)(dst + 4) = f32x4{bf_lo(st[2]), bf_hi(st[2]), bf_lo(st[3]), bf_hi(st[3])};
    lds[L_Q + tid] = __uint_as_float(qw);
    if (AMLATE) LDS_BARRIER();
    unsigned am = 0;
#pragma unroll
    for (int k = 0; k < 4; ++k) am = max(am, max((st[k] << 16) & 0x7fff0000u, st[k] & 0x7fff0000u));
    am = umax32(am);
    am = max(am, (unsigned)__builtin_amdgcn_readlane((int)am, 32));
    if (lane == 0) lds[L_AMAX + wave] = __uint_as_float(am);
  }
  if (!AMLATE) LDS_BARRIER();
  STAMP(2);
  u32x4 qb[2];
  {
    const u32x2* qp = (const u32x2*)(lds + L_Q + (i >> 1) * (D / 2) + (2 * g + hf) * 2);
    const u32x2 q0 = qp[0], q1 = qp[8], q2 = qp[16], q3 = qp[24];
    qb[0] = u32x4{q0[0], q0[1], q1[0], q1[1]}; qb[1] = u32x4{q2[0], q2[1], q3[0], q3[1]};
  }
  float s[16];
#pragma unroll
  for (int T = 0; T < 16; ++T) s[T] = 0.f;
#pragma unroll 1
  for (int it = 0; it < NSTEP; ++it) {
    const bool last = it == NSTEP - 1;
    auto rP = __builtin_amdgcn_make_buffer_rsrc((void*)((last ? qv : qk) + tile0), 0, RBYTES, RSRC);
    const int pvo = last ? voffV : voffK, pso = SOFF(it + 1), psa = last ? 512 : 128;
    const float* kc = lds + L_CONST + it * 128 + (2 * g + hf) * 4;
#pragma unroll
    for (int T = 0; T < 14; ++T) s[T] = s[T + 2];
    f32x4 ks[4], kz[4];
#pragma unroll
    for (int p = 0; p < 4; ++p) { ks[p] = *(const f32x4*)(kc + p * 32); kz[p] = *(const f32x4*)(kc + NSTEP * 128 + p * 32); }
    SB();
#pragma unroll
    for (int u = 0; u < 2; ++u) {
      f32x4 d = {0.f, 0.f, 0.f, 0.f};
#pragma unroll
      for (int m = 0; m < 2; ++m) {
        u32x4 a;
#pragma unroll
        for (int e = 0; e < 2; ++e) {
          const int p = 2 * m + e, j = 4 * u + p;
          const u32x4 x = win[j];
          a[2 * e + 0] = deq2(x[0], x[1], f32x2{kz[p][0], kz[p][1]}, f32x2{ks[p][0], ks[p][1]});
          a[2 * e + 1] = deq2(x[2], x[3], f32x2{kz[p][2], kz[p][3]}, f32x2{ks[p][2], ks[p][3]});
          SB();
          THROTTLE(THR); ISSUE(j, rP, pvo, pso, psa);
          SB();
        }
        d = __builtin_amdgcn_mfma_f32_16x16x32_bf16(__builtin_bit_cast(bf16x8, a), __builtin_bit_cast(bf16x8, qb[m]), d, 0, 0, 0);
      }
      const float mine = hf ? d[3] : d[0], send = hf ? d[1] : d[2];
      const float dot = mine + dpp<X1>(send);
      s[14 + u] = rnd_bf16(rnd_bf16(dot) * 0.088388347648318440f);
    }
    STAMP(8 + it);
    if (!last) { SB(); __builtin_amdgcn_s_barrier(); SB(); }
  }
  STAMP(3);
  asm volatile("" :: "v"(warm));
  float mx = s[0];
#pragma unroll
  for (int T = 1; T < 16; ++T) mx = fmaxf(mx, s[T]);
  mx = fmaxf(mx, dpp<X1>(mx));
  mx = xrow_max(mx);
  if (lane < 16 && hf == 0) lds[L_MX + (i >> 1) * NWAVE + wave] = mx;
  LDS_BARRIER();
  {
    const f32x4 m0 = *(const f32x4*)(lds + L_MX + (i >> 1) * NWAVE), m1 = *(const f32x4*)(lds + L_MX + (i >> 1) * NWAVE + 4);
    mx = fmaxf(fmaxf(fmaxf(m0[0], m0[1]), fmaxf(m0[2], m0[3])), fmaxf(fmaxf(m1[0], m1[1]), fmaxf(m1[2], m1[3])));
  }
  float lsum = 0.f;
  unsigned pa[16];
#pragma unroll
  for (int T = 0; T < 16; ++T) {
    const float p = __builtin_amdgcn_exp2f((s[T] - mx) * LOG2E);
    lsum += p;
    pa[T] = pk_bf16(hf ? 0.f : p, hf ? p : 0.f);
  }
  lsum += dpp<X1>(lsum);
  lsum = xrow_sum(lsum);
  if (lane < 16 && hf == 0) lds[L_SUM + (i >> 1) * NWAVE + wave] = lsum;
  LDS_BARRIER();
  const unsigned tagA = (epoch + 1u) * 0x9E3779B1u, tagB = ~tagA;
  {
    const f32x4 l0 = *(const f32x4*)(lds + L_SUM + (i >> 1) * NWAVE), l1 = *(const f32x4*)(lds + L_SUM + (i >> 1) * NWAVE + 4);
    const u32x4 am = *(const u32x4*)(lds + L_AMAX + 4);
    const float L = ((l0[0] + l0[1]) + (l0[2] + l0[3])) + ((l1[0] + l1[1]) + (l1[2] + l1[3]));
    const float vb = (1.f + __uint_as_float(max(am[2], am[3])) * 0x1p-31f) * __uint_as_float(max(am[0], am[1]));
    __builtin_amdgcn_raw_buffer_store_b128(u32x4{__float_as_uint(mx), tagA, __float_as_uint(L), (tagB & ~0xffu) | ((__float_as_uint(vb) >> 23) & 0xffu)}, rW,
                                           (tid < 16 && hf == 0) ? (i >> 1) * (MLT ? 512 : 16) : OOB, WS_ML + bh * (NSPLIT * 128) + split * (MLT ? 16 : 128), AUX_SC1);
  }
  const float m_own = __int_as_float(__builtin_amdgcn_readlane(__float_as_int(mx), 2 * wave));
  f32x4 acc[8];
#pragma unroll
  for (int k = 0; k < 8; ++k) acc[k] = f32x4{0.f, 0.f, 0.f, 0.f};
  const int so_ml = WS_ML + bh * (NSPLIT * 128) + wave * (MLT ? 512 : 16), vo_ml = (lane & 31) * (MLT ? 16 : 128);
  u32x4 ml = {0u, 0u, 0u, 0u};
  float fac = 0.f;
  unsigned ebound = 0;
  bool bad = true;
  {
    auto rV = __builtin_amdgcn_make_buffer_rsrc((void*)(qv + tile0), 0, RBYTES, RSRC);
#pragma unroll 1
    for (int it = 0; it < NSTEP; ++it) {
      const float* vc = lds + L_CONST + (2 * NSTEP + it) * 128 + wave * 16 + 4 * g;
      const int pso = SOFF(it + 1);
      const int pvo = it == NSTEP - 1 ? OOB : voffV;
      const f32x4 vs = *(const f32x4*)(vc), vz = *(const f32x4*)(vc + NSTEP * 128);
      SB();
      fac = out_factor(ml, m_own);
      ebound = umax32(ml[3] & 0xffu);
      bad = ((ml[1] ^ tagA) | ((ml[3] ^ tagB) >> 8)) != 0u;
      asm volatile("" : "+v"(fac), "+v"(ebound));
      SB();
      ml = __builtin_amdgcn_raw_buffer_load_b128(rW, it == NSTEP - 2 ? vo_ml : OOB, so_ml, AUX_SC1);
      SB();
      const u32x2 pp = {pa[0], pa[1]};
      u32x2 bv[8];
#pragma unroll
      for (int u = 0; u < 2; ++u)
#pragma unroll
        for (int dh = 0; dh < 2; ++dh) {
          const int j = 4 * u + 2 * dh;
          const u32x4 x0 = win[j], x1 = win[j + 1];
#pragma unroll
          for (int c = 0; c < 4; ++c) bv[dh * 4 + c][u] = deq2(x0[c], x1[c], f32x2{vz[2 * u], vz[2 * u + 1]}, f32x2{vs[2 * u], vs[2 * u + 1]});
          if (u == 1) {
#pragma unroll
            for (int k = 4 * dh; k < 4 * dh + 4; ++k)
              acc[k] = __builtin_amdgcn_mfma_f32_16x16x16bf16_1k(__builtin_bit_cast(s16x4, pp), __builtin_bit_cast(s16x4, bv[k]), acc[k], 0, 0, 0);
          }
          SB();
          THROTTLE(THRV); ISSUE(j, rV, pvo, pso, 512); SB();
          THROTTLE(THRV2); ISSUE(j + 1, rV, pvo, pso, 512); SB();
        }
      STAMP(16 + it);
      if (it != NSTEP - 1) { SB(); __builtin_amdgcn_s_barrier(); SB(); }
#pragma unroll
      for (int T = 0; T < 14; ++T) pa[T] = pa[T + 2];
    }
  }
  STAMP(4);
  float* my = lds + L_MRG + wave * (REP * D);
#pragma unroll
  for (int u = 0; u < 2; ++u)
#pragma unroll
    for (int dh = 0; dh < 2; ++dh) {
      f32x4 o;
#pragma unroll
      for (int c = 0; c < 4; ++c) o[c] = acc[dh * 4 + c][2 * u] + acc[dh * 4 + c][2 * u + 1];
      *(f32x4*)(my + (2 * g + u) * D + 64 * dh + 4 * i) = o;
    }
  if (__builtin_expect(__builtin_amdgcn_ballot_w64(bad) != 0ull, 0)) {
#pragma unroll 1
    for (unsigned spins = 0; spins < (1u << 20); ++spins) {
      asm volatile("" ::: "memory");
      ml = __builtin_amdgcn_raw_buffer_load_b128(rW, vo_ml, so_ml, AUX_SC1);
      if (__builtin_amdgcn_ballot_w64(((ml[1] ^ tagA) | ((ml[3] ^ tagB) >> 8)) != 0u) == 0ull) break;
      __builtin_amdgcn_s_sleep(8);
    }
    fac = out_factor(ml, m_own);
    ebound = umax32(ml[3] & 0xffu);
  }
  const int kexp = 151 - (int)ebound;
  unsigned long long* acc64 = (unsigned long long*)((char*)ws + WS_ACC) + (bh * REP + wave) * D + lane;
  auto rO = __builtin_amdgcn_make_buffer_rsrc((void*)out, 0, B * HQ * D * 2, RSRC);
  LDS_BARRIER();
  {
    float o0 = 0.f, o1 = 0.f;
#pragma unroll
    for (int w = 0; w < NWAVE; ++w) { o0 += lds[L_MRG + w * (REP * D) + wave * D + lane]; o1 += lds[L_MRG + w * (REP * D) + wave * D + 64 + lane]; }
    o0 *= fac; o1 *= fac;
    const long long a0 = ((long long)__builtin_rint(__builtin_ldexp((double)o0, kexp)) << 6) + 1;
    const long long a1 = ((long long)__builtin_rint(__builtin_ldexp((double)o1, kexp)) << 6) + 1;
    STAMP(5);
    const long long r0 = (long long)__hip_atomic_fetch_add(acc64, (unsigned long long)a0, __ATOMIC_RELAXED, __HIP_MEMORY_SCOPE_AGENT) + a0;
    const long long r1 = (long long)__hip_atomic_fetch_add(acc64 + 64, (unsigned long long)a1, __ATOMIC_RELAXED, __HIP_MEMORY_SCOPE_AGENT) + a1;
    const float nanp = ebound == 255u ? __uint_as_float(0x7fc00000u) : fac - fac;
    const unsigned pk = pk_bf16((float)__builtin_ldexp((double)(r0 >> 6), -kexp) + nanp, (float)__builtin_ldexp((double)(r1 >> 6), -kexp) + nanp);
    STAMP(6);
    __builtin_amdgcn_raw_buffer_store_b16((unsigned short)pk, rO, ((unsigned)r0 & 63u) == NSPLIT ? lane * 2 : OOB, (bh * REP + wave) * 256, AUX_SC1);
    __builtin_amdgcn_raw_buffer_store_b16((unsigned short)(pk >> 16), rO, ((unsigned)r1 & 63u) == NSPLIT ? lane * 2 + 128 : OOB, (bh * REP + wave) * 256, AUX_SC1);
    __builtin_amdgcn_raw_buffer_store_b32(epoch + 1u, rW, (tid | split) == 0 ? 0 : OOB, bh * 256, AUX_SC1);
    if (CODEPF) asm volatile(".Lattn_tail_mark:");
    STAMP(7);
  }
  STAMP_OUT(wg * 8 + wave);
}

extern "C" void kernel_launch(void* const* d_in, const int* in_sizes, int n_in, void* d_out, int out_size, void* d_ws, size_t ws_size,
                              hipStream_t stream) {
  attn_main<<<NBH * NSPLIT, 512, 0, stream>>>((const unsigned short*)d_in[0], (const int*)d_in[1], (const int*)d_in[2],
                                              (const unsigned short*)d_in[3], (const unsigned short*)d_in[4],
                                              (const unsigned short*)d_in[5], (const unsigned short*)d_in[6], (unsigned*)d_ws, (unsigned short*)d_out);
}
```

```cpp
#include <hip/hip_runtime.h>
#include <hip/hip_bf16.h>
#include <stdint.h>

typedef unsigned u32x4 __attribute__((ext_vector_type(4)));
typedef unsigned u32x2 __attribute__((ext_vector_type(2)));
typedef float    f32x4 __attribute__((ext_vector_type(4)));
typedef float    f32x2 __attribute__((ext_vector_type(2)));
typedef __bf16   bf16x2 __attribute__((ext_vector_type(2)));
typedef __bf16   bf16x8 __attribute__((ext_vector_type(8)));
typedef short    s16x4 __attribute__((ext_vector_type(4)));

constexpr int AUX_NT = 2, AUX_SC1 = 16, RSRC = 0x00020000;
constexpr int OOB = 0x40000000;
constexpr int B = 2, HQ = 32, HKV = 4, SKV = 32768, D = 128, G = 128, NG = SKV / G, REP = HQ / HKV;
constexpr int NBH = B * HKV, NWAVE = 8, NSPLIT = 32, NSTEP = NG / NSPLIT;
constexpr int STEP_BYTES = NSPLIT * G * D * 4;
constexpr float LOG2E = 1.4426950408889634f, SCALE = 0.088388347648318440f;
constexpr int WS_ML = 4096, WS_ACC = WS_ML + NBH * REP * NSPLIT * 16, WS_END = WS_ACC + NBH * REP * D * 8;
constexpr int L_CONST = 0, L_Q = 4 * NSTEP * 128, L_MX = L_Q + REP * D / 2, L_SUM = L_MX + NWAVE * REP, L_AMAX = L_SUM + NWAVE * REP,
              L_MRG = L_AMAX + NWAVE, L_END = L_MRG + NWAVE * REP * D;

__device__ __forceinline__ unsigned pk_bf16(float lo, float hi) {
  f32x2 v = {lo, hi};
  return __builtin_bit_cast(unsigned, __builtin_convertvector(v, bf16x2));
}
__device__ __forceinline__ float bf_lo(unsigned u) { return __builtin_bit_cast(float, u << 16); }
__device__ __forceinline__ float bf_hi(unsigned u) { return __builtin_bit_cast(float, u & 0xffff0000u); }
__device__ __forceinline__ float rnd_bf16(float x) { return __builtin_bit_cast(float, pk_bf16(0.f, x)); }
template <int CTRL> __device__ __forceinline__ unsigned dppu(unsigned x) { return (unsigned)__builtin_amdgcn_mov_dpp((int)x, CTRL, 0xf, 0xf, true); }
template <int CTRL> __device__ __forceinline__ float dpp(float x) { return __uint_as_float(dppu<CTRL>(__float_as_uint(x))); }
constexpr int X1 = 0xB1, X2 = 0x4E, X7 = 0x141, X8 = 0x128;
__device__ __forceinline__ float x16_max(float x) {
  auto s = __builtin_amdgcn_permlane16_swap(__float_as_uint(x), __float_as_uint(x), false, false);
  return fmaxf(__uint_as_float(s[0]), __uint_as_float(s[1]));
}
__device__ __forceinline__ float x16_sum(float x) {
  auto s = __builtin_amdgcn_permlane16_swap(__float_as_uint(x), __float_as_uint(x), false, false);
  return __uint_as_float(s[0]) + __uint_as_float(s[1]);
}
__device__ __forceinline__ float xrow_max(float x) {
  x = x16_max(x);
  auto t = __builtin_amdgcn_permlane32_swap(__float_as_uint(x), __float_as_uint(x), false, false);
  return fmaxf(__uint_as_float(t[0]), __uint_as_float(t[1]));
}
__device__ __forceinline__ float xrow_sum(float x) {
  x = x16_sum(x);
  auto t = __builtin_amdgcn_permlane32_swap(__float_as_uint(x), __float_as_uint(x), false, false);
  return __uint_as_float(t[0]) + __uint_as_float(t[1]);
}
__device__ __forceinline__ unsigned umax32(unsigned x) {
  x = max(x, dppu<X1>(x)); x = max(x, dppu<X2>(x)); x = max(x, dppu<X7>(x)); x = max(x, dppu<X8>(x));
  auto s = __builtin_amdgcn_permlane16_swap(x, x, false, false);
  return max(s[0], s[1]);
}
__device__ __forceinline__ unsigned deq2(unsigned c0, unsigned c1, f32x2 z, f32x2 s) {
  f32x2 x = {(float)(int)c0, (float)(int)c1};
  x -= z;
  f32x2 r = {rnd_bf16(x[0]), rnd_bf16(x[1])};
  r *= s;
  return pk_bf16(r[0], r[1]);
}
__device__ __forceinline__ float out_factor(u32x4 ml, float m_own) {
  const float m = __uint_as_float(ml[0]);
  float M = fmaxf(m, dpp<X1>(m)); M = fmaxf(M, dpp<X2>(M)); M = fmaxf(M, dpp<X7>(M)); M = fmaxf(M, dpp<X8>(M)); M = x16_max(M);
  float L = __builtin_amdgcn_exp2f((m - M) * LOG2E) * __uint_as_float(ml[2]);
  L += dpp<X1>(L); L += dpp<X2>(L); L += dpp<X7>(L); L += dpp<X8>(L); L = x16_sum(L);
  return __builtin_amdgcn_exp2f((m_own - M) * LOG2E) / L;
}
#define SB() __builtin_amdgcn_sched_barrier(0)
#define LDS_BARRIER() do { asm volatile("s_waitcnt lgkmcnt(0)" ::: "memory"); __builtin_amdgcn_s_barrier(); asm volatile("" ::: "memory"); } while (0)

__global__ __launch_bounds__(512, 2) void kvq_decode_kernel(const unsigned short* __restrict__ q, const int* __restrict__ qk, const int* __restrict__ qv,
                                                            const unsigned short* __restrict__ ksc, const unsigned short* __restrict__ kzr,
                                                            const unsigned short* __restrict__ vsc, const unsigned short* __restrict__ vzr,
                                                            unsigned* ws, unsigned short* __restrict__ out) {
  __shared__ __attribute__((aligned(16))) float lds[L_END];
  const unsigned tid = threadIdx.x, lane = tid & 63, wave = __builtin_amdgcn_readfirstlane(tid >> 6);
  const unsigned bh = blockIdx.x % NBH, split = blockIdx.x / NBH;
  const unsigned i = lane & 15, g = lane >> 4, hf = i & 1;
#define SOFF(st) ((((st) + bh) & 7) * STEP_BYTES)
  const size_t tile0 = (size_t)((bh * SKV + split * G + wave * 16) * D);
  constexpr int RBYTES = (NSTEP - 1) * STEP_BYTES + 16 * D * 4;
  const int voffK = (4 * (i >> 2) + ((i >> 1) & 1)) * 512 + (2 * g + hf) * 16;
  const int voffV = 4 * g * 512 + i * 16;
  const int odd_xcd = (int)(__builtin_amdgcn_s_getreg(20 | (3 << 11)) & 1);
  auto rW = __builtin_amdgcn_make_buffer_rsrc((void*)ws, 0, WS_END, RSRC);

  u32x4 win[8];
#define ISSUE(j, rs, vo, so, sa) win[j] = __builtin_amdgcn_raw_buffer_load_b128(rs, (vo) + ((j) >> 2) * 1024 + (((j) >> 1) & 1) * 256, (so) + ((j) & 1) * (sa), AUX_NT)
#define THROTTLE() do { asm volatile("s_cmp_lg_u32 %0, 0\n\ts_cbranch_scc1 1f\n\ts_waitcnt vmcnt(3)\n1:" :: "s"(odd_xcd) : "scc"); SB(); } while (0)
  unsigned epoch;
  {
    auto rK = __builtin_amdgcn_make_buffer_rsrc((void*)(qk + tile0), 0, RBYTES, RSRC);
    const unsigned arr = wave >> 1, stp = (wave & 1) * 4 + g;
    const unsigned short* src = arr == 0 ? ksc : arr == 1 ? kzr : arr == 2 ? vsc : vzr;
    const u32x4 st = __builtin_amdgcn_raw_buffer_load_b128(__builtin_amdgcn_make_buffer_rsrc((void*)src, 0, NBH * NG * 256, RSRC),
                                                           ((stp + bh) & 7) * (NSPLIT * 256) + i * 16, (bh * NG + split) * 256, 0);
    const unsigned qw = __builtin_amdgcn_raw_buffer_load_b32(__builtin_amdgcn_make_buffer_rsrc((void*)q, 0, B * HQ * D * 2, RSRC), tid * 4, bh * (REP * D * 2), 0);
    epoch = __builtin_amdgcn_raw_buffer_load_b32(rW, 0, bh * 256, AUX_SC1);
    __builtin_amdgcn_raw_buffer_store_b128(u32x4{0u, 0u, 0u, 0u}, rW, tid - 7 * 64 < 16 ? lane * 16 : OOB, WS_ACC + (bh * NSPLIT + split) * 256, AUX_SC1);
    SB();
    asm volatile("s_cmp_lg_u32 %0, 7\n\ts_cbranch_scc1 1f\n\ts_waitcnt vmcnt(0)\n1:" :: "s"(wave) : "scc", "memory");
    SB();
#pragma unroll
    for (int j = 0; j < 8; ++j) { ISSUE(j, rK, voffK, SOFF(0), 128); SB(); }
    float* dst = lds + L_CONST + (arr * NSTEP + stp) * 128 + i * 8;
    *(f32x4*)dst = f32x4{bf_lo(st[0]), bf_hi(st[0]), bf_lo(st[1]), bf_hi(st[1])};
    *(f32x4*)(dst + 4) = f32x4{bf_lo(st[2]), bf_hi(st[2]), bf_lo(st[3]), bf_hi(st[3])};
    lds[L_Q + tid] = __uint_as_float(qw);
    LDS_BARRIER();
    unsigned am = 0;
#pragma unroll
    for (int k = 0; k < 4; ++k) am = max(am, max((st[k] << 16) & 0x7fff0000u, st[k] & 0x7fff0000u));
    am = umax32(am);
    am = max(am, (unsigned)__builtin_amdgcn_readlane((int)am, 32));
    if (lane == 0) lds[L_AMAX + wave] = __uint_as_float(am);
  }
  u32x4 qb[2];
  {
    const u32x2* qp = (const u32x2*)(lds + L_Q + (i >> 1) * (D / 2) + (2 * g + hf) * 2);
    const u32x2 q0 = qp[0], q1 = qp[8], q2 = qp[16], q3 = qp[24];
    qb[0] = u32x4{q0[0], q0[1], q1[0], q1[1]}; qb[1] = u32x4{q2[0], q2[1], q3[0], q3[1]};
  }
  float s[16];
#pragma unroll
  for (int T = 0; T < 16; ++T) s[T] = 0.f;
#pragma unroll 1
  for (int it = 0; it < NSTEP; ++it) {
    const bool last = it == NSTEP - 1;
    auto rP = __builtin_amdgcn_make_buffer_rsrc((void*)((last ? qv : qk) + tile0), 0, RBYTES, RSRC);
    const int pvo = last ? voffV : voffK, pso = SOFF(it + 1), psa = last ? 512 : 128;
    const float* kc = lds + L_CONST + it * 128 + (2 * g + hf) * 4;
#pragma unroll
    for (int T = 0; T < 14; ++T) s[T] = s[T + 2];
    f32x4 ks[4], kz[4];
#pragma unroll
    for (int p = 0; p < 4; ++p) { ks[p] = *(const f32x4*)(kc + p * 32); kz[p] = *(const f32x4*)(kc + NSTEP * 128 + p * 32); }
    SB();
#pragma unroll
    for (int u = 0; u < 2; ++u) {
      f32x4 d = {0.f, 0.f, 0.f, 0.f};
#pragma unroll
      for (int m = 0; m < 2; ++m) {
        u32x4 a;
#pragma unroll
        for (int e = 0; e < 2; ++e) {
          const int p = 2 * m + e, j = 4 * u + p;
          const u32x4 x = win[j];
          a[2 * e + 0] = deq2(x[0], x[1], f32x2{kz[p][0], kz[p][1]}, f32x2{ks[p][0], ks[p][1]});
          a[2 * e + 1] = deq2(x[2], x[3], f32x2{kz[p][2], kz[p][3]}, f32x2{ks[p][2], ks[p][3]});
          SB();
          THROTTLE(); ISSUE(j, rP, pvo, pso, psa);
          SB();
        }
        d = __builtin_amdgcn_mfma_f32_16x16x32_bf16(__builtin_bit_cast(bf16x8, a), __builtin_bit_cast(bf16x8, qb[m]), d, 0, 0, 0);
      }
      const float mine = hf ? d[3] : d[0], send = hf ? d[1] : d[2];
      s[14 + u] = rnd_bf16(rnd_bf16(mine + dpp<X1>(send)) * SCALE);
    }
    if (!last) { SB(); __builtin_amdgcn_s_barrier(); SB(); }
  }
  float mx = s[0];
#pragma unroll
  for (int T = 1; T < 16; ++T) mx = fmaxf(mx, s[T]);
  mx = fmaxf(mx, dpp<X1>(mx));
  mx = xrow_max(mx);
  if (lane < 16 && hf == 0) lds[L_MX + (i >> 1) * NWAVE + wave] = mx;
  LDS_BARRIER();
  {
    const f32x4 m0 = *(const f32x4*)(lds + L_MX + (i >> 1) * NWAVE), m1 = *(const f32x4*)(lds + L_MX + (i >> 1) * NWAVE + 4);
    mx = fmaxf(fmaxf(fmaxf(m0[0], m0[1]), fmaxf(m0[2], m0[3])), fmaxf(fmaxf(m1[0], m1[1]), fmaxf(m1[2], m1[3])));
  }
  float lsum = 0.f;
  unsigned pa[16];
#pragma unroll
  for (int T = 0; T < 16; ++T) {
    const float p = __builtin_amdgcn_exp2f((s[T] - mx) * LOG2E);
    lsum += p;
    pa[T] = pk_bf16(hf ? 0.f : p, hf ? p : 0.f);
    asm volatile("" : "+v"(pa[T]));
  }
  lsum += dpp<X1>(lsum);
  lsum = xrow_sum(lsum);
  if (lane < 16 && hf == 0) lds[L_SUM + (i >> 1) * NWAVE + wave] = lsum;
  LDS_BARRIER();
  const unsigned tagA = (epoch + 1u) * 0x9E3779B1u, tagB = ~tagA;
  {
    const f32x4 l0 = *(const f32x4*)(lds + L_SUM + (i >> 1) * NWAVE), l1 = *(const f32x4*)(lds + L_SUM + (i >> 1) * NWAVE + 4);
    const u32x4 am = *(const u32x4*)(lds + L_AMAX + 4);
    const float L = ((l0[0] + l0[1]) + (l0[2] + l0[3])) + ((l1[0] + l1[1]) + (l1[2] + l1[3]));
    const float vb = (1.f + __uint_as_float(max(am[2], am[3])) * 0x1p-31f) * __uint_as_float(max(am[0], am[1]));
    __builtin_amdgcn_raw_buffer_store_b128(u32x4{__float_as_uint(mx), tagA, __float_as_uint(L), (tagB & ~0xffu) | ((__float_as_uint(vb) >> 23) & 0xffu)}, rW,
                                           (tid < 16 && hf == 0) ? (i >> 1) * (NSPLIT * 16) : OOB, WS_ML + bh * (REP * NSPLIT * 16) + split * 16, AUX_SC1);
  }
  const float m_own = __int_as_float(__builtin_amdgcn_readlane(__float_as_int(mx), 2 * wave));
  f32x4 acc[8];
#pragma unroll
  for (int k = 0; k < 8; ++k) acc[k] = f32x4{0.f, 0.f, 0.f, 0.f};
  const int so_ml = WS_ML + (bh * REP + wave) * (NSPLIT * 16), vo_ml = (lane & 31) * 16;
  u32x4 ml;
  float fac = 0.f;
  unsigned ebound = 0;
  bool bad = true;
  {
    auto rV = __builtin_amdgcn_make_buffer_rsrc((void*)(qv + tile0), 0, RBYTES, RSRC);
#pragma unroll 1
    for (int it = 0; it < NSTEP; ++it) {
      const float* vc = lds + L_CONST + (2 * NSTEP + it) * 128 + wave * 16 + 4 * g;
      const int pso = SOFF(it + 1);
      const int pvo = it == NSTEP - 1 ? OOB : voffV;
      const f32x4 vs = *(const f32x4*)(vc), vz = *(const f32x4*)(vc + NSTEP * 128);
      SB();
      ml = __builtin_amdgcn_raw_buffer_load_b128(rW, it == NSTEP - 2 ? vo_ml : OOB, so_ml, AUX_SC1);
      SB();
      u32x2 bv[8];
#pragma unroll
      for (int u = 0; u < 2; ++u)
#pragma unroll
        for (int dh = 0; dh < 2; ++dh) {
          const int j = 4 * u + 2 * dh;
          const u32x4 x0 = win[j], x1 = win[j + 1];
#pragma unroll
          for (int c = 0; c < 4; ++c) {
            unsigned y = deq2(x0[c], x1[c], f32x2{vz[2 * u], vz[2 * u + 1]}, f32x2{vs[2 * u], vs[2 * u + 1]});
            asm volatile("" : "+v"(y));
            bv[dh * 4 + c][u] = y;
          }
          SB();
          THROTTLE(); ISSUE(j, rV, pvo, pso, 512); SB();
          ISSUE(j + 1, rV, pvo, pso, 512); SB();
        }
      const u32x2 pp = {pa[0], pa[1]};
#pragma unroll
      for (int k = 0; k < 8; ++k)
        acc[k] = __builtin_amdgcn_mfma_f32_16x16x16bf16_1k(__builtin_bit_cast(s16x4, pp), __builtin_bit_cast(s16x4, bv[k]), acc[k], 0, 0, 0);
#pragma unroll
      for (int T = 0; T < 14; ++T) pa[T] = pa[T + 2];
      if (it == NSTEP - 2) {
        fac = out_factor(ml, m_own);
        ebound = umax32(ml[3] & 0xffu);
        bad = ((ml[1] ^ tagA) | ((ml[3] ^ tagB) >> 8)) != 0u;
        asm volatile("" : "+v"(fac), "+v"(ebound));
      }
      if (it != NSTEP - 1) { SB(); __builtin_amdgcn_s_barrier(); SB(); }
    }
  }
  float* my = lds + L_MRG + wave * (REP * D);
#pragma unroll
  for (int u = 0; u < 2; ++u)
#pragma unroll
    for (int dh = 0; dh < 2; ++dh) {
      f32x4 o;
#pragma unroll
      for (int c = 0; c < 4; ++c) o[c] = acc[dh * 4 + c][2 * u] + acc[dh * 4 + c][2 * u + 1];
      *(f32x4*)(my + (2 * g + u) * D + 64 * dh + 4 * i) = o;
    }
  if (__builtin_expect(__builtin_amdgcn_ballot_w64(bad) != 0ull, 0)) {
#pragma unroll 1
    for (unsigned spins = 0; spins < (1u << 20); ++spins) {
      asm volatile("" ::: "memory");
      ml = __builtin_amdgcn_raw_buffer_load_b128(rW, vo_ml, so_ml, AUX_SC1);
      if (__builtin_amdgcn_ballot_w64(((ml[1] ^ tagA) | ((ml[3] ^ tagB) >> 8)) != 0u) == 0ull) break;
      __builtin_amdgcn_s_sleep(8);
    }
    fac = out_factor(ml, m_own);
    ebound = umax32(ml[3] & 0xffu);
  }
  const int kexp = 151 - (int)ebound;
  float nanp = ebound == 255u ? __uint_as_float(0x7fc00000u) : fac - fac;
  asm volatile("" : "+v"(nanp));
  unsigned long long* acc64 = (unsigned long long*)((char*)ws + WS_ACC) + (bh * REP + wave) * D + lane;
  auto rO = __builtin_amdgcn_make_buffer_rsrc((void*)out, 0, B * HQ * D * 2, RSRC);
  LDS_BARRIER();
  f32x2 t[NWAVE];
#pragma unroll
  for (int w = 0; w < NWAVE; ++w) t[w] = f32x2{lds[L_MRG + w * (REP * D) + wave * D + lane], lds[L_MRG + w * (REP * D) + wave * D + 64 + lane]};
  SB();
  const f32x2 o = (((t[0] + t[1]) + (t[2] + t[3])) + ((t[4] + t[5]) + (t[6] + t[7]))) * fac;
  const long long a0 = (long long)((unsigned long long)(long long)__builtin_rint(__builtin_ldexp((double)o[0], kexp)) << 6) + 1;
  const long long a1 = (long long)((unsigned long long)(long long)__builtin_rint(__builtin_ldexp((double)o[1], kexp)) << 6) + 1;
  const long long r0 = (long long)(__hip_atomic_fetch_add(acc64, (unsigned long long)a0, __ATOMIC_RELAXED, __HIP_MEMORY_SCOPE_AGENT) + (unsigned long long)a0);
  const long long r1 = (long long)(__hip_atomic_fetch_add(acc64 + 64, (unsigned long long)a1, __ATOMIC_RELAXED, __HIP_MEMORY_SCOPE_AGENT) + (unsigned long long)a1);
  const unsigned pk = pk_bf16((float)__builtin_ldexp((double)(r0 >> 6), -kexp) + nanp, (float)__builtin_ldexp((double)(r1 >> 6), -kexp) + nanp);
  __builtin_amdgcn_raw_buffer_store_b16((unsigned short)pk, rO, ((unsigned)r0 & 63u) == NSPLIT ? lane * 2 : OOB, (bh * REP + wave) * (D * 2), AUX_SC1);
  __builtin_amdgcn_raw_buffer_store_b16((unsigned short)(pk >> 16), rO, ((unsigned)r1 & 63u) == NSPLIT ? lane * 2 + 128 : OOB, (bh * REP + wave) * (D * 2), AUX_SC1);
  __builtin_amdgcn_raw_buffer_store_b32(epoch + 1u, rW, (tid | split) == 0 ? 0 : OOB, bh * 256, AUX_SC1);
}

extern "C" void kernel_launch(void* const* d_in, const int* in_sizes, int n_in, void* d_out, int out_size, void* d_ws, size_t ws_size,
                              hipStream_t stream) {
  kvq_decode_kernel<<<NBH * NSPLIT, NWAVE * 64, 0, stream>>>((const unsigned short*)d_in[0], (const int*)d_in[1], (const int*)d_in[2],
                                                             (const unsigned short*)d_in[3], (const unsigned short*)d_in[4],
                                                             (const unsigned short*)d_in[5], (const unsigned short*)d_in[6],
                                                             (unsigned*)d_ws, (unsigned short*)d_out);
}
```

```cpp
#include <hip/hip_runtime.h>
#include <hip/hip_bf16.h>
#include <stdint.h>

typedef unsigned u32x4 __attribute__((ext_vector_type(4)));
typedef unsigned u32x2 __attribute__((ext_vector_type(2)));
typedef float    f32x4 __attribute__((ext_vector_type(4)));
typedef float    f32x2 __attribute__((ext_vector_type(2)));
typedef __bf16   bf16x2 __attribute__((ext_vector_type(2)));
typedef __bf16   bf16x8 __attribute__((ext_vector_type(8)));
typedef short    s16x4 __attribute__((ext_vector_type(4)));

constexpr int AUX_NT = 2, AUX_SC1 = 16, RSRC = 0x00020000;
constexpr int OOB = 0x40000000;
constexpr int B = 2, HQ = 32, HKV = 4, SKV = 32768, D = 128, G = 128, NG = SKV / G, REP = HQ / HKV;
constexpr int NBH = B * HKV, NWAVE = 8, NSPLIT = 32, NSTEP = NG / NSPLIT;
constexpr int STEP_BYTES = NSPLIT * G * D * 4;
constexpr float LOG2E = 1.4426950408889634f, SCALE = 0.088388347648318440f;
constexpr int WS_ML = 4096, WS_ACC = WS_ML + NBH * REP * NSPLIT * 16, WS_END = WS_ACC + NBH * REP * D * 8;
constexpr int L_CONST = 0, L_Q = 4 * NSTEP * 128, L_MX = L_Q + REP * D / 2, L_SUM = L_MX + NWAVE * REP, L_AMAX = L_SUM + NWAVE * REP,
              L_MRG = L_AMAX + NWAVE, L_END = L_MRG + NWAVE * REP * D;

__device__ __forceinline__ unsigned pk_bf16(float lo, float hi) {
  f32x2 v = {lo, hi};
  return __builtin_bit_cast(unsigned, __builtin_convertvector(v, bf16x2));
}
__device__ __forceinline__ float bf_lo(unsigned u) { return __builtin_bit_cast(float, u << 16); }
__device__ __forceinline__ float bf_hi(unsigned u) { return __builtin_bit_cast(float, u & 0xffff0000u); }
__device__ __forceinline__ float rnd_bf16(float x) { return __builtin_bit_cast(float, pk_bf16(0.f, x)); }
template <int CTRL> __device__ __forceinline__ unsigned dppu(unsigned x) { return (unsigned)__builtin_amdgcn_mov_dpp((int)x, CTRL, 0xf, 0xf, true); }
template <int CTRL> __device__ __forceinline__ float dpp(float x) { return __uint_as_float(dppu<CTRL>(__float_as_uint(x))); }
constexpr int X1 = 0xB1, X2 = 0x4E, X7 = 0x141, X8 = 0x128;
__device__ __forceinline__ float x16_max(float x) {
  auto s = __builtin_amdgcn_permlane16_swap(__float_as_uint(x), __float_as_uint(x), false, false);
  return fmaxf(__uint_as_float(s[0]), __uint_as_float(s[1]));
}
__device__ __forceinline__ float x16_sum(float x) {
  auto s = __builtin_amdgcn_permlane16_swap(__float_as_uint(x), __float_as_uint(x), false, false);
  return __uint_as_float(s[0]) + __uint_as_float(s[1]);
}
__device__ __forceinline__ float xrow_max(float x) {
  x = x16_max(x);
  auto t = __builtin_amdgcn_permlane32_swap(__float_as_uint(x), __float_as_uint(x), false, false);
  return fmaxf(__uint_as_float(t[0]), __uint_as_float(t[1]));
}
__device__ __forceinline__ float xrow_sum(float x) {
  x = x16_sum(x);
  auto t = __builtin_amdgcn_permlane32_swap(__float_as_uint(x), __float_as_uint(x), false, false);
  return __uint_as_float(t[0]) + __uint_as_float(t[1]);
}
__device__ __forceinline__ unsigned umax32(unsigned x) {
  x = max(x, dppu<X1>(x)); x = max(x, dppu<X2>(x)); x = max(x, dppu<X7>(x)); x = max(x, dppu<X8>(x));
  auto s = __builtin_amdgcn_permlane16_swap(x, x, false, false);
  return max(s[0], s[1]);
}
__device__ __forceinline__ unsigned deq2(unsigned c0, unsigned c1, f32x2 z, f32x2 s) {
  f32x2 x = {(float)(int)c0, (float)(int)c1};
  x -= z;
  f32x2 r = {rnd_bf16(x[0]), rnd_bf16(x[1])};
  r *= s;
  return pk_bf16(r[0], r[1]);
}
__device__ __forceinline__ float out_factor(u32x4 ml, float m_own) {
  const float m = __uint_as_float(ml[0]);
  float M = fmaxf(m, dpp<X1>(m)); M = fmaxf(M, dpp<X2>(M)); M = fmaxf(M, dpp<X7>(M)); M = fmaxf(M, dpp<X8>(M)); M = x16_max(M);
  float L = __builtin_amdgcn_exp2f((m - M) * LOG2E) * __uint_as_float(ml[2]);
  L += dpp<X1>(L); L += dpp<X2>(L); L += dpp<X7>(L); L += dpp<X8>(L); L = x16_sum(L);
  return __builtin_amdgcn_exp2f((m_own - M) * LOG2E) / L;
}
#define SB() __builtin_amdgcn_sched_barrier(0)
#define LDS_BARRIER() do { asm volatile("s_waitcnt lgkmcnt(0)" ::: "memory"); __builtin_amdgcn_s_barrier(); asm volatile("" ::: "memory"); } while (0)

__global__ __launch_bounds__(512, 2) void kvq_decode_kernel(const unsigned short* __restrict__ q, const int* __restrict__ qk, const int* __restrict__ qv,
                                                            const unsigned short* __restrict__ ksc, const unsigned short* __restrict__ kzr,
                                                            const unsigned short* __restrict__ vsc, const unsigned short* __restrict__ vzr,
                                                            unsigned* ws, unsigned short* __restrict__ out) {
  __shared__ __attribute__((aligned(16))) float lds[L_END];
  const unsigned tid = threadIdx.x, lane = tid & 63, wave = __builtin_amdgcn_readfirstlane(tid >> 6);
  const unsigned bh = blockIdx.x % NBH, split = blockIdx.x / NBH;
  const unsigned i = lane & 15, g = lane >> 4, hf = i & 1;
  const int tile0 = (bh * SKV + split * G + wave * 16) * (D * 4);
#define SOFF(st) (tile0 + (((st) + bh) & 7) * STEP_BYTES)
  constexpr int RBYTES = NBH * SKV * D * 4;
  const int voffK = (4 * (i >> 2) + ((i >> 1) & 1)) * 512 + (2 * g + hf) * 16;
  const int voffV = 4 * g * 512 + i * 16;
  const int odd_xcd = (int)(__builtin_amdgcn_s_getreg(20 | (3 << 11)) & 1);
  auto rW = __builtin_amdgcn_make_buffer_rsrc((void*)ws, 0, WS_END, RSRC);

  u32x4 win[8];
#define ISSUE(j, rs, vo, so, sa) win[j] = __builtin_amdgcn_raw_buffer_load_b128(rs, (vo) + ((j) >> 2) * 1024 + (((j) >> 1) & 1) * 256, (so) + ((j) & 1) * (sa), AUX_NT)
#define THROTTLE(n) do { asm volatile("s_cmp_lg_u32 %0, 0\n\ts_cbranch_scc1 1f\n\ts_waitcnt vmcnt(" #n ")\n1:" :: "s"(odd_xcd) : "scc"); SB(); } while (0)
  unsigned epoch;
  {
    auto rK = __builtin_amdgcn_make_buffer_rsrc((void*)qk, 0, RBYTES, RSRC);
    const unsigned arr = wave >> 1, stp = (wave & 1) * 4 + g;
    const unsigned short* src = arr == 0 ? ksc : arr == 1 ? kzr : arr == 2 ? vsc : vzr;
    const u32x4 st = __builtin_amdgcn_raw_buffer_load_b128(__builtin_amdgcn_make_buffer_rsrc((void*)src, 0, NBH * NG * 256, RSRC),
                                                           ((stp + bh) & 7) * (NSPLIT * 256) + i * 16, (bh * NG + split) * 256, 0);
    const unsigned qw = __builtin_amdgcn_raw_buffer_load_b32(__builtin_amdgcn_make_buffer_rsrc((void*)q, 0, B * HQ * D * 2, RSRC), tid * 4, bh * (REP * D * 2), 0);
    epoch = __builtin_amdgcn_raw_buffer_load_b32(rW, 0, bh * 256, AUX_SC1);
    __builtin_amdgcn_raw_buffer_store_b128(u32x4{0u, 0u, 0u, 0u}, rW, tid - 7 * 64 < 16 ? lane * 16 : OOB, WS_ACC + (bh * NSPLIT + split) * 256, AUX_SC1);
    SB();
    asm volatile("s_cmp_lg_u32 %0, 7\n\ts_cbranch_scc1 1f\n\ts_waitcnt vmcnt(0)\n1:" :: "s"(wave) : "scc", "memory");
    SB();
#pragma unroll
    for (int j = 0; j < 8; ++j) { ISSUE(j, rK, voffK, SOFF(0), 128); SB(); }
    float* dst = lds + L_CONST + (arr * NSTEP + stp) * 128 + i * 8;
    *(f32x4*)dst = f32x4{bf_lo(st[0]), bf_hi(st[0]), bf_lo(st[1]), bf_hi(st[1])};
    *(f32x4*)(dst + 4) = f32x4{bf_lo(st[2]), bf_hi(st[2]), bf_lo(st[3]), bf_hi(st[3])};
    lds[L_Q + tid] = __uint_as_float(qw);
    LDS_BARRIER();
    unsigned am = 0;
#pragma unroll
    for (int k = 0; k < 4; ++k) am = max(am, max((st[k] << 16) & 0x7fff0000u, st[k] & 0x7fff0000u));
    am = umax32(am);
    am = max(am, (unsigned)__builtin_amdgcn_readlane((int)am, 32));
    if (lane == 0) lds[L_AMAX + wave] = __uint_as_float(am);
  }
  u32x4 qb[2];
  {
    const u32x2* qp = (const u32x2*)(lds + L_Q + (i >> 1) * (D / 2) + (2 * g + hf) * 2);
    const u32x2 q0 = qp[0], q1 = qp[8], q2 = qp[16], q3 = qp[24];
    qb[0] = u32x4{q0[0], q0[1], q1[0], q1[1]}; qb[1] = u32x4{q2[0], q2[1], q3[0], q3[1]};
  }
  float s[16];
#pragma unroll
  for (int T = 0; T < 16; ++T) s[T] = 0.f;
#pragma unroll 1
  for (int it = 0; it < NSTEP; ++it) {
    const bool last = it == NSTEP - 1;
    auto rP = __builtin_amdgcn_make_buffer_rsrc((void*)(last ? qv : qk), 0, RBYTES, RSRC);
    const int pvo = last ? voffV : voffK, pso = SOFF(it + 1), psa = last ? 512 : 128;
    const float* kc = lds + L_CONST + it * 128 + (2 * g + hf) * 4;
#pragma unroll
    for (int T = 0; T < 14; ++T) s[T] = s[T + 2];
    f32x4 ks[4], kz[4];
#pragma unroll
    for (int p = 0; p < 4; ++p) { ks[p] = *(const f32x4*)(kc + p * 32); kz[p] = *(const f32x4*)(kc + NSTEP * 128 + p * 32); }
    SB();
#pragma unroll
    for (int u = 0; u < 2; ++u) {
      f32x4 d = {0.f, 0.f, 0.f, 0.f};
#pragma unroll
      for (int m = 0; m < 2; ++m) {
        u32x4 a;
#pragma unroll
        for (int e = 0; e < 2; ++e) {
          const int p = 2 * m + e, j = 4 * u + p;
          const u32x4 x = win[j];
          a[2 * e + 0] = deq2(x[0], x[1], f32x2{kz[p][0], kz[p][1]}, f32x2{ks[p][0], ks[p][1]});
          a[2 * e + 1] = deq2(x[2], x[3], f32x2{kz[p][2], kz[p][3]}, f32x2{ks[p][2], ks[p][3]});
          SB();
          THROTTLE(3); ISSUE(j, rP, pvo, pso, psa);
          SB();
        }
        d = __builtin_amdgcn_mfma_f32_16x16x32_bf16(__builtin_bit_cast(bf16x8, a), __builtin_bit_cast(bf16x8, qb[m]), d, 0, 0, 0);
      }
      const float mine = hf ? d[3] : d[0], send = hf ? d[1] : d[2];
      s[14 + u] = rnd_bf16(rnd_bf16(mine + dpp<X1>(send)) * SCALE);
    }
    if (!last) { SB(); __builtin_amdgcn_s_barrier(); SB(); }
  }
  float mx = s[0];
#pragma unroll
  for (int T = 1; T < 16; ++T) mx = fmaxf(mx, s[T]);
  mx = fmaxf(mx, dpp<X1>(mx));
  mx = xrow_max(mx);
  if (lane < 16 && hf == 0) lds[L_MX + (i >> 1) * NWAVE + wave] = mx;
  LDS_BARRIER();
  {
    const f32x4 m0 = *(const f32x4*)(lds + L_MX + (i >> 1) * NWAVE), m1 = *(const f32x4*)(lds + L_MX + (i >> 1) * NWAVE + 4);
    mx = fmaxf(fmaxf(fmaxf(m0[0], m0[1]), fmaxf(m0[2], m0[3])), fmaxf(fmaxf(m1[0], m1[1]), fmaxf(m1[2], m1[3])));
  }
  float lsum = 0.f;
  unsigned pa[16];
#pragma unroll
  for (int T = 0; T < 16; ++T) {
    const float p = __builtin_amdgcn_exp2f((s[T] - mx) * LOG2E);
    lsum += p;
    pa[T] = pk_bf16(hf ? 0.f : p, hf ? p : 0.f);
    asm volatile("" : "+v"(pa[T]));
  }
  lsum += dpp<X1>(lsum);
  lsum = xrow_sum(lsum);
  if (lane < 16 && hf == 0) lds[L_SUM + (i >> 1) * NWAVE + wave] = lsum;
  LDS_BARRIER();
  const unsigned tagA = (epoch + 1u) * 0x9E3779B1u, tagB = ~tagA;
  {
    const f32x4 l0 = *(const f32x4*)(lds + L_SUM + (i >> 1) * NWAVE), l1 = *(const f32x4*)(lds + L_SUM + (i >> 1) * NWAVE + 4);
    const u32x4 am = *(const u32x4*)(lds + L_AMAX + 4);
    const float L = ((l0[0] + l0[1]) + (l0[2] + l0[3])) + ((l1[0] + l1[1]) + (l1[2] + l1[3]));
    const float vb = (1.f + __uint_as_float(max(am[2], am[3])) * 0x1p-31f) * __uint_as_float(max(am[0], am[1]));
    __builtin_amdgcn_raw_buffer_store_b128(u32x4{__float_as_uint(mx), tagA, __float_as_uint(L), (tagB & ~0xffu) | ((__float_as_uint(vb) >> 23) & 0xffu)}, rW,
                                           (tid < 16 && hf == 0) ? (i >> 1) * (NSPLIT * 16) : OOB, WS_ML + bh * (REP * NSPLIT * 16) + split * 16, AUX_SC1);
  }
  const float m_own = __int_as_float(__builtin_amdgcn_readlane(__float_as_int(mx), 2 * wave));
  f32x4 acc[8];
#pragma unroll
  for (int k = 0; k < 8; ++k) acc[k] = f32x4{0.f, 0.f, 0.f, 0.f};
  const int so_ml = WS_ML + (bh * REP + wave) * (NSPLIT * 16), vo_ml = (lane & 31) * 16;
  u32x4 ml;
  float fac = 0.f;
  unsigned ebound = 0;
  bool bad = true;
  {
    auto rV = __builtin_amdgcn_make_buffer_rsrc((void*)qv, 0, RBYTES, RSRC);
#pragma unroll 1
    for (int it = 0; it < NSTEP; ++it) {
      const float* vc = lds + L_CONST + (2 * NSTEP + it) * 128 + wave * 16 + 4 * g;
      const int pso = SOFF(it + 1);
      const int pvo = it == NSTEP - 1 ? OOB : voffV;
      const f32x4 vs = *(const f32x4*)(vc), vz = *(const f32x4*)(vc + NSTEP * 128);
      SB();
      ml = __builtin_amdgcn_raw_buffer_load_b128(rW, it == NSTEP - 2 ? vo_ml : OOB, so_ml, AUX_SC1);
      SB();
      u32x2 bv[8];
#pragma unroll
      for (int u = 0; u < 2; ++u)
#pragma unroll
        for (int dh = 0; dh < 2; ++dh) {
          const int j = 4 * u + 2 * dh;
          const u32x4 x0 = win[j], x1 = win[j + 1];
#pragma unroll
          for (int c = 0; c < 4; ++c) {
            unsigned y = deq2(x0[c], x1[c], f32x2{vz[2 * u], vz[2 * u + 1]}, f32x2{vs[2 * u], vs[2 * u + 1]});
            asm volatile("" : "+v"(y));
            bv[dh * 4 + c][u] = y;
          }
          SB();
          THROTTLE(2); ISSUE(j, rV, pvo, pso, 512); SB();
          ISSUE(j + 1, rV, pvo, pso, 512); SB();
        }
      const u32x2 pp = {pa[0], pa[1]};
#pragma unroll
      for (int k = 0; k < 8; ++k)
        acc[k] = __builtin_amdgcn_mfma_f32_16x16x16bf16_1k(__builtin_bit_cast(s16x4, pp), __builtin_bit_cast(s16x4, bv[k]), acc[k], 0, 0, 0);
#pragma unroll
      for (int T = 0; T < 14; ++T) pa[T] = pa[T + 2];
      if (it == NSTEP - 2) {
        fac = out_factor(ml, m_own);
        ebound = umax32(ml[3] & 0xffu);
        bad = ((ml[1] ^ tagA) | ((ml[3] ^ tagB) >> 8)) != 0u;
        asm volatile("" : "+v"(fac), "+v"(ebound));
      }
      if (it != NSTEP - 1) { SB(); __builtin_amdgcn_s_barrier(); SB(); }
    }
  }
  float* my = lds + L_MRG + wave * (REP * D);
#pragma unroll
  for (int u = 0; u < 2; ++u)
#pragma unroll
    for (int dh = 0; dh < 2; ++dh) {
      f32x4 o;
#pragma unroll
      for (int c = 0; c < 4; ++c) o[c] = acc[dh * 4 + c][2 * u] + acc[dh * 4 + c][2 * u + 1];
      *(f32x4*)(my + (2 * g + u) * D + 64 * dh + 4 * i) = o;
    }
  if (__builtin_expect(__builtin_amdgcn_ballot_w64(bad) != 0ull, 0)) {
#pragma unroll 1
    for (unsigned spins = 0; spins < (1u << 20); ++spins) {
      asm volatile("" ::: "memory");
      ml = __builtin_amdgcn_raw_buffer_load_b128(rW, vo_ml, so_ml, AUX_SC1);
      if (__builtin_amdgcn_ballot_w64(((ml[1] ^ tagA) | ((ml[3] ^ tagB) >> 8)) != 0u) == 0ull) break;
      __builtin_amdgcn_s_sleep(8);
    }
    fac = out_factor(ml, m_own);
    ebound = umax32(ml[3] & 0xffu);
  }
  const int kexp = 151 - (int)ebound;
  float nanp = ebound == 255u ? __uint_as_float(0x7fc00000u) : fac - fac;
  asm volatile("" : "+v"(nanp));
  unsigned long long* acc64 = (unsigned long long*)((char*)ws + WS_ACC) + (bh * REP + wave) * D + lane;
  auto rO = __builtin_amdgcn_make_buffer_rsrc((void*)out, 0, B * HQ * D * 2, RSRC);
  LDS_BARRIER();
  f32x2 t[NWAVE];
#pragma unroll
  for (int w = 0; w < NWAVE; ++w) t[w] = f32x2{lds[L_MRG + w * (REP * D) + wave * D + lane], lds[L_MRG + w * (REP * D) + wave * D + 64 + lane]};
  SB();
  const f32x2 o = (((t[0] + t[1]) + (t[2] + t[3])) + ((t[4] + t[5]) + (t[6] + t[7]))) * fac;
  const long long a0 = (long long)((unsigned long long)(long long)__builtin_rint(__builtin_ldexp((double)o[0], kexp)) << 6) + 1;
  const long long a1 = (long long)((unsigned long long)(long long)__builtin_rint(__builtin_ldexp((double)o[1], kexp)) << 6) + 1;
  const long long r0 = (long long)(__hip_atomic_fetch_add(acc64, (unsigned long long)a0, __ATOMIC_RELAXED, __HIP_MEMORY_SCOPE_AGENT) + (unsigned long long)a0);
  const long long r1 = (long long)(__hip_atomic_fetch_add(acc64 + 64, (unsigned long long)a1, __ATOMIC_RELAXED, __HIP_MEMORY_SCOPE_AGENT) + (unsigned long long)a1);
  const unsigned pk = pk_bf16((float)__builtin_ldexp((double)(r0 >> 6), -kexp) + nanp, (float)__builtin_ldexp((double)(r1 >> 6), -kexp) + nanp);
  __builtin_amdgcn_raw_buffer_store_b16((unsigned short)pk, rO, ((unsigned)r0 & 63u) == NSPLIT ? lane * 2 : OOB, (bh * REP + wave) * (D * 2), AUX_SC1);
  __builtin_amdgcn_raw_buffer_store_b16((unsigned short)(pk >> 16), rO, ((unsigned)r1 & 63u) == NSPLIT ? lane * 2 + 128 : OOB, (bh * REP + wave) * (D * 2), AUX_SC1);
  __builtin_amdgcn_raw_buffer_store_b32(epoch + 1u, rW, (tid | split) == 0 ? 0 : OOB, bh * 256, AUX_SC1);
}

extern "C" void kernel_launch(void* const* d_in, const int* in_sizes, int n_in, void* d_out, int out_size, void* d_ws, size_t ws_size,
                              hipStream_t stream) {
  kvq_decode_kernel<<<NBH * NSPLIT, NWAVE * 64, 0, stream>>>((const unsigned short*)d_in[0], (const int*)d_in[1], (const int*)d_in[2],
                                                             (const unsigned short*)d_in[3], (const unsigned short*)d_in[4],
                                                             (const unsigned short*)d_in[5], (const unsigned short*)d_in[6],
                                                             (unsigned*)d_ws, (unsigned short*)d_out);
}
```

```cpp
#include <hip/hip_runtime.h>
#include <hip/hip_bf16.h>
#include <stdint.h>

typedef unsigned u32x4 __attribute__((ext_vector_type(4)));
typedef unsigned u32x2 __attribute__((ext_vector_type(2)));
typedef int      i32x4 __attribute__((ext_vector_type(4)));
typedef float    f32x4 __attribute__((ext_vector_type(4)));
typedef float    f32x2 __attribute__((ext_vector_type(2)));
typedef __bf16   bf16x2 __attribute__((ext_vector_type(2)));
typedef __bf16   bf16x8 __attribute__((ext_vector_type(8)));
typedef volatile __attribute__((address_space(3))) unsigned lds_word;

constexpr int AUX_SC1 = 16, RSRC = 0x00020000;
constexpr int OOB = 0x40000000;
constexpr int B = 2, HQ = 32, HKV = 4, SKV = 32768, D = 128, G = 128, NG = SKV / G, REP = HQ / HKV;
constexpr int NBH = B * HKV, NWAVE = 8, NSPLIT = 32, NSTEP = NG / NSPLIT;
constexpr int STEP_BYTES = NSPLIT * G * D * 4;
constexpr float LOG2E = 1.4426950408889634f, SCALE = 0.088388347648318440f;
constexpr int INFL = 21, INFL_LAST = 31;
static_assert(INFL < 24 && INFL_LAST < 32, "the loader publishes a slot after 24 (32) loads of the next one");
constexpr int WS_ML = 4096, WS_ACC = WS_ML + NBH * NSPLIT * REP * 16, WS_END = WS_ACC + NBH * REP * D * 8;
constexpr int NSLOT = 4, SLOT_BYTES = 32768, PROW = NSTEP * G + 8;
constexpr int L_VC = 0, L_MX = 2 * NSTEP * 128, L_SUM = L_MX + NWAVE * REP, L_AMAX = L_SUM + NWAVE * REP, L_FAC = L_AMAX + NWAVE,
              L_CTL = L_FAC + REP, L_ML = L_CTL + 16, L_KC = L_ML + REP * NSPLIT * 4, L_Q = L_KC + 2 * NSTEP * 128, L_P = L_KC,
              L_RING = L_P + REP * PROW / 2, L_END = L_RING + NSLOT * SLOT_BYTES / 4;
static_assert(L_Q + REP * D / 2 <= L_RING && L_KC % 4 == 0 && L_RING % 4 == 0 && L_END * 4 <= 160 * 1024, "LDS map");
constexpr int C_FULL = NWAVE, C_CNT = NWAVE + 1;

__device__ __forceinline__ unsigned pk_bf16(float lo, float hi) {
  f32x2 v = {lo, hi};
  return __builtin_bit_cast(unsigned, __builtin_convertvector(v, bf16x2));
}
__device__ __forceinline__ float bf_lo(unsigned u) { return __builtin_bit_cast(float, u << 16); }
__device__ __forceinline__ float bf_hi(unsigned u) { return __builtin_bit_cast(float, u & 0xffff0000u); }
__device__ __forceinline__ float rnd_bf16(float x) { return __builtin_bit_cast(float, pk_bf16(0.f, x)); }
template <int CTRL> __device__ __forceinline__ unsigned dppu(unsigned x) { return (unsigned)__builtin_amdgcn_mov_dpp((int)x, CTRL, 0xf, 0xf, true); }
template <int CTRL> __device__ __forceinline__ float dpp(float x) { return __uint_as_float(dppu<CTRL>(__float_as_uint(x))); }
constexpr int X1 = 0xB1, X2 = 0x4E, X7 = 0x141, X8 = 0x128;
__device__ __forceinline__ float xrow_max(float x) {
  auto s = __builtin_amdgcn_permlane16_swap(__float_as_uint(x), __float_as_uint(x), false, false);
  x = fmaxf(__uint_as_float(s[0]), __uint_as_float(s[1]));
  auto t = __builtin_amdgcn_permlane32_swap(__float_as_uint(x), __float_as_uint(x), false, false);
  return fmaxf(__uint_as_float(t[0]), __uint_as_float(t[1]));
}
__device__ __forceinline__ float xrow_sum(float x) {
  auto s = __builtin_amdgcn_permlane16_swap(__float_as_uint(x), __float_as_uint(x), false, false);
  x = __uint_as_float(s[0]) + __uint_as_float(s[1]);
  auto t = __builtin_amdgcn_permlane32_swap(__float_as_uint(x), __float_as_uint(x), false, false);
  return __uint_as_float(t[0]) + __uint_as_float(t[1]);
}
__device__ __forceinline__ unsigned umax64(unsigned x) {
  x = max(x, dppu<X1>(x)); x = max(x, dppu<X2>(x)); x = max(x, dppu<X7>(x)); x = max(x, dppu<X8>(x));
  auto s = __builtin_amdgcn_permlane16_swap(x, x, false, false);
  x = max(s[0], s[1]);
  auto t = __builtin_amdgcn_permlane32_swap(x, x, false, false);
  return max(t[0], t[1]);
}
__device__ __forceinline__ unsigned deq2(unsigned c0, unsigned c1, f32x2 z, f32x2 s) {
  f32x2 x = {(float)(int)c0, (float)(int)c1};
  x -= z;
  f32x2 r = {rnd_bf16(x[0]), rnd_bf16(x[1])};
  r *= s;
  return pk_bf16(r[0], r[1]);
}
__device__ __forceinline__ float out_factor(float x, float m_own) {
  const float m = dpp<0xA0>(x), l = dpp<0xF5>(x);
  float M = fmaxf(m, dpp<X2>(m)); M = fmaxf(M, dpp<X7>(M)); M = fmaxf(M, dpp<X8>(M)); M = xrow_max(M);
  float L = __builtin_amdgcn_exp2f((m - M) * LOG2E) * l;
  L += dpp<X2>(L); L += dpp<X7>(L); L += dpp<X8>(L); L = xrow_sum(L);
  return __builtin_amdgcn_exp2f((m_own - M) * LOG2E) / L;
}

__device__ __forceinline__ void loader(const int* qk, const int* qv, unsigned ring, lds_word* ctl, int group0, unsigned bh, unsigned lane) {
  const int voff0 = (lane >> 5) * 512 + ((lane & 31) ^ (4 * (lane >> 5))) * 16;
  const unsigned a_full = (unsigned)(size_t)(ctl + C_FULL), a_rel = (unsigned)(size_t)(ctl + (lane & 7));
  unsigned slot = 0, rel = 0;
  i32x4 rs = {0, 0, NBH * SKV * D * 4, RSRC};
#define LD "s_waitcnt vmcnt(%c[n])\n\tbuffer_load_dwordx4 %[v], %[rs], %[so] offen nt lds\n\t"
#define INC(c) "s_addk_i32 m0, " c "\n\ts_addk_i32 %[so], " c "\n\t"
#define FOUR LD INC("0x1000") LD INC("0x1000") LD INC("0x1000") LD
#pragma unroll 1
  for (unsigned h = 0; h < 4 * NSTEP; ++h) {
    const bool isv = h >= 2 * NSTEP;
    const unsigned long long p = (unsigned long long)(isv ? qv : qk);
    rs[0] = (int)p; rs[1] = (int)(p >> 32) & 0xffff;
    int so = group0 + (((h >> 1) + bh) & 7) * STEP_BYTES + (h & 1) * SLOT_BYTES;
    while (__builtin_expect(__builtin_amdgcn_ballot_w64(rel + (NSLOT - 1) < h) != 0ull, 0)) rel = ctl[lane & 7];
    if (__builtin_expect(h == 4 * NSTEP - 1, 0)) {
#pragma unroll 1
      for (int k = 0; k < 8; ++k) {
        const int first = (k >> 2) * 16384 + (k & 3) * 1024;
        int so4 = so + first;
        asm volatile("s_mov_b32 m0, %[lb]\n\t" FOUR : [so] "+s"(so4) : [lb] "s"(ring + slot + first), [v] "v"(voff0), [rs] "s"(rs), [n] "i"(INFL_LAST) : "scc", "memory");
      }
      ctl[C_FULL] = 2 * h;
    } else {
      int vcur = voff0, cnt;
      asm volatile("s_mov_b32 m0, %[lb]\n\ts_mov_b32 %[cnt], 0\n"
                   "1:\n\t" FOUR INC("0x1000") FOUR INC("0x9400")
                   "v_xor_b32 %[v], %[xm], %[v]\n\ts_add_u32 %[cnt], %[cnt], 1\n\ts_cmp_lg_u32 %[cnt], 3\n\ts_cbranch_scc1 2f\n\t"
                   "ds_write_b32 %[af], %[fv]\n\tds_read_b32 %[rel], %[ar]\n"
                   "2:\n\ts_cmp_lt_u32 %[cnt], 4\n\ts_cbranch_scc1 1b\n\ts_waitcnt lgkmcnt(0)"
                   : [so] "+s"(so), [rel] "=&v"(rel), [v] "+v"(vcur), [cnt] "=&s"(cnt)
                   : [lb] "s"(ring + slot), [xm] "s"(isv ? 0 : 128), [rs] "s"(rs), [af] "v"(a_full), [fv] "v"(2 * h), [ar] "v"(a_rel), [n] "i"(INFL)
                   : "scc", "memory");
    }
    slot = slot == (NSLOT - 1) * SLOT_BYTES ? 0 : slot + SLOT_BYTES;
  }
  asm volatile("s_waitcnt vmcnt(16)" ::: "memory");
  ctl[C_FULL] = 8 * NSTEP - 1;
  asm volatile("s_waitcnt vmcnt(0)" ::: "memory");
  __builtin_amdgcn_s_barrier();
}

__global__ __launch_bounds__(576, 3) void kvq_decode_kernel(const unsigned short* __restrict__ q, const int* __restrict__ qk, const int* __restrict__ qv,
                                                            const unsigned short* __restrict__ ksc, const unsigned short* __restrict__ kzr,
                                                            const unsigned short* __restrict__ vsc, const unsigned short* __restrict__ vzr,
                                                            unsigned* ws, unsigned short* __restrict__ out) {
  __shared__ __attribute__((aligned(16))) float lds[L_END];
  const unsigned lane = threadIdx.x & 63, wave9 = __builtin_amdgcn_readfirstlane(threadIdx.x >> 6);
  const unsigned bh = blockIdx.x % NBH, split = blockIdx.x / NBH;
  lds_word* ctl = (lds_word*)(lds + L_CTL);
  if (threadIdx.x < 12) ctl[lane] = 0u;
  asm volatile("" :: "s"(q), "s"(qk), "s"(qv), "s"(ksc), "s"(kzr), "s"(vsc), "s"(vzr), "s"(ws));
  asm volatile("s_waitcnt lgkmcnt(0)" ::: "memory");
  __builtin_amdgcn_s_barrier();
  asm volatile("" ::: "memory");
  if (__builtin_expect(wave9 == 0, 1)) {
    __builtin_amdgcn_s_setprio(3);
    loader(qk, qv, (unsigned)(size_t)(__attribute__((address_space(3))) float*)(lds + L_RING), ctl, (bh * SKV + split * G) * (D * 4), bh, lane);
    return;
  }
  const unsigned tid = threadIdx.x - 64, wave = wave9 - 1;
  const unsigned i = lane & 15, g = lane >> 4, hf = i & 1;
  auto rW = __builtin_amdgcn_make_buffer_rsrc((void*)ws, 0, WS_END, RSRC);
#define WAIT_FULL(n) do { while (__builtin_amdgcn_readfirstlane(ctl[C_FULL]) < (n)) __builtin_amdgcn_s_sleep(1); asm volatile("" ::: "memory"); } while (0)
#define RELEASE(n) do { asm volatile("s_waitcnt lgkmcnt(0)" ::: "memory"); ctl[wave] = (n); } while (0)
#define MEET(k) do { asm volatile("s_waitcnt lgkmcnt(0)" ::: "memory"); \
    if (lane == 0) __hip_atomic_fetch_add((unsigned*)(lds + L_CTL + C_CNT), 1u, __ATOMIC_RELAXED, __HIP_MEMORY_SCOPE_WORKGROUP); \
    while (__builtin_amdgcn_readfirstlane(ctl[C_CNT]) < NWAVE * (k)) __builtin_amdgcn_s_sleep(1); asm volatile("" ::: "memory"); } while (0)
  unsigned epoch;
  {
    const unsigned arr = wave >> 1, stp = (wave & 1) * 4 + g;
    const unsigned short* src = arr == 0 ? ksc : arr == 1 ? kzr : arr == 2 ? vsc : vzr;
    const u32x4 st = __builtin_amdgcn_raw_buffer_load_b128(__builtin_amdgcn_make_buffer_rsrc((void*)src, 0, NBH * NG * 256, RSRC),
                                                           ((stp + bh) & 7) * (NSPLIT * 256) + i * 16, (bh * NG + split) * 256, 0);
    const unsigned qw = __builtin_amdgcn_raw_buffer_load_b32(__builtin_amdgcn_make_buffer_rsrc((void*)q, 0, B * HQ * D * 2, RSRC), tid * 4, bh * (REP * D * 2), 0);
    epoch = __builtin_amdgcn_raw_buffer_load_b32(rW, 0, bh * 256, AUX_SC1);
    __builtin_amdgcn_raw_buffer_store_b128(u32x4{0u, 0u, 0u, 0u}, rW, tid < 16 ? lane * 16 : OOB, WS_ACC + (bh * NSPLIT + split) * 256, AUX_SC1);
    float* dst = lds + (arr < 2 ? L_KC : L_VC) + ((arr & 1) * NSTEP + stp) * 128;
    if (arr < 2) {
      *(f32x4*)(dst + i * 8) = f32x4{bf_lo(st[0]), bf_hi(st[0]), bf_lo(st[1]), bf_hi(st[1])};
      *(f32x4*)(dst + i * 8 + 4) = f32x4{bf_lo(st[2]), bf_hi(st[2]), bf_lo(st[3]), bf_hi(st[3])};
    } else {
      *(f32x4*)(dst + (i >> 1) * 16 + hf * 4) = f32x4{bf_lo(st[0]), bf_lo(st[1]), bf_lo(st[2]), bf_lo(st[3])};
      *(f32x4*)(dst + (i >> 1) * 16 + hf * 4 + 8) = f32x4{bf_hi(st[0]), bf_hi(st[1]), bf_hi(st[2]), bf_hi(st[3])};
    }
    lds[L_Q + tid] = __uint_as_float(qw);
    unsigned am = 0;
#pragma unroll
    for (int k = 0; k < 4; ++k) am = max(am, max((st[k] << 16) & 0x7fff0000u, st[k] & 0x7fff0000u));
    am = umax64(am);
    if (lane == 0) lds[L_AMAX + wave] = __uint_as_float(am);
  }
  unsigned slot = 0;
#define NEXT_SLOT() (slot = slot == (NSLOT - 1) * SLOT_BYTES ? 0 : slot + SLOT_BYTES)
  float s[16];
#pragma unroll
  for (int T = 0; T < 16; ++T) s[T] = 0.f;
  {
    u32x4 qb[2] = {{0u, 0u, 0u, 0u}, {0u, 0u, 0u, 0u}};
    const unsigned tt = i >> 1, ch = (2 * g + hf) ^ (4 * (tt & 3));
    const char* k0 = (const char*)(lds + L_RING) + (wave * 8 + tt) * 512 + ch * 16;
    const char* k1 = (const char*)(lds + L_RING) + (wave * 8 + tt) * 512 + (ch ^ 8) * 16;
#pragma unroll 1
    for (int it = 0; it < NSTEP; ++it) {
      const float* kc = lds + L_KC + it * 128 + (2 * g + hf) * 4;
#pragma unroll
      for (int T = 0; T < 14; ++T) s[T] = s[T + 2];
      f32x4 ks[4], kz[4];
#pragma unroll
      for (int u = 0; u < 2; ++u) {
        WAIT_FULL(4 * it + 2 * u + 2);
        u32x4 x[4];
#pragma unroll
        for (int p = 0; p < 4; ++p) x[p] = *(const u32x4*)(((p & 1) ? k1 : k0) + slot + (p >> 1) * 256);
        RELEASE(2 * it + u + 1);
        asm volatile("" : "+v"(x[0]), "+v"(x[1]), "+v"(x[2]), "+v"(x[3]) :: "memory");
        NEXT_SLOT();
        if (u == 0) {
          if (it == 0) {
            MEET(1);
            const u32x2* qp = (const u32x2*)(lds + L_Q + (i >> 1) * (D / 2) + (2 * g + hf) * 2);
            const u32x2 q0 = qp[0], q1 = qp[8], q2 = qp[16], q3 = qp[24];
            qb[0] = u32x4{q0[0], q0[1], q1[0], q1[1]}; qb[1] = u32x4{q2[0], q2[1], q3[0], q3[1]};
          }
#pragma unroll
          for (int p = 0; p < 4; ++p) { ks[p] = *(const f32x4*)(kc + p * 32); kz[p] = *(const f32x4*)(kc + NSTEP * 128 + p * 32); }
        }
        f32x4 d = {0.f, 0.f, 0.f, 0.f};
#pragma unroll
        for (int m = 0; m < 2; ++m) {
          u32x4 a;
#pragma unroll
          for (int e = 0; e < 2; ++e) {
            const int p = 2 * m + e;
            a[2 * e + 0] = deq2(x[p][0], x[p][1], f32x2{kz[p][0], kz[p][1]}, f32x2{ks[p][0], ks[p][1]});
            a[2 * e + 1] = deq2(x[p][2], x[p][3], f32x2{kz[p][2], kz[p][3]}, f32x2{ks[p][2], ks[p][3]});
          }
          d = __builtin_amdgcn_mfma_f32_16x16x32_bf16(__builtin_bit_cast(bf16x8, a), __builtin_bit_cast(bf16x8, qb[m]), d, 0, 0, 0);
        }
        const float mine = hf ? d[3] : d[0], send = hf ? d[1] : d[2];
        s[14 + u] = rnd_bf16(rnd_bf16(mine + dpp<X1>(send)) * SCALE);
        asm volatile("" : "+v"(s[14 + u]));
      }
    }
  }
  float mx = s[0];
#pragma unroll
  for (int T = 1; T < 16; ++T) mx = fmaxf(mx, s[T]);
  mx = fmaxf(mx, dpp<X1>(mx));
  mx = xrow_max(mx);
  if (lane < 16 && hf == 0) lds[L_MX + (i >> 1) * NWAVE + wave] = mx;
  MEET(2);
  {
    const f32x4 m0 = *(const f32x4*)(lds + L_MX + (i >> 1) * NWAVE), m1 = *(const f32x4*)(lds + L_MX + (i >> 1) * NWAVE + 4);
    mx = fmaxf(fmaxf(fmaxf(m0[0], m0[1]), fmaxf(m0[2], m0[3])), fmaxf(fmaxf(m1[0], m1[1]), fmaxf(m1[2], m1[3])));
  }
  float lsum = 0.f;
  {
    unsigned short* pw = (unsigned short*)(lds + L_P) + (i >> 1) * PROW + (wave >> 1) * 16 + hf * 8 + (wave & 1) * 4 + g;
#pragma unroll
    for (int T = 0; T < 16; ++T) {
      const float p = __builtin_amdgcn_exp2f((s[T] - mx) * LOG2E);
      lsum += p;
      pw[T * 64] = (unsigned short)pk_bf16(p, p);
    }
  }
  lsum += dpp<X1>(lsum);
  lsum = xrow_sum(lsum);
  if (lane < 16 && hf == 0) lds[L_SUM + (i >> 1) * NWAVE + wave] = lsum;
  const unsigned tag = ((epoch + 1u) * 0x9E3779B1u) ^ (0u - hf), paymask = hf ? 0xffu : 0u;
  const float m_own = __int_as_float(__builtin_amdgcn_readlane(__float_as_int(mx), 2 * wave));
  asm volatile("s_waitcnt vmcnt(0)" ::: "memory");
  MEET(3);
  if (wave == 0) {
    const f32x4 l0 = *(const f32x4*)(lds + L_SUM + (i >> 1) * NWAVE), l1 = *(const f32x4*)(lds + L_SUM + (i >> 1) * NWAVE + 4);
    const u32x4 am = *(const u32x4*)(lds + L_AMAX + 4);
    const float L = ((l0[0] + l0[1]) + (l0[2] + l0[3])) + ((l1[0] + l1[1]) + (l1[2] + l1[3]));
    const float vb = (1.f + __uint_as_float(max(am[2], am[3])) * 0x1p-31f) * __uint_as_float(max(am[0], am[1]));
    __builtin_amdgcn_raw_buffer_store_b64(u32x2{__float_as_uint(hf ? L : mx), (tag & ~paymask) | ((__float_as_uint(vb) >> 23) & paymask)}, rW,
                                          lane < 16 ? i * 8 : OOB, WS_ML + (bh * NSPLIT + split) * 128, AUX_SC1);
  }
  f32x4 acc = {0.f, 0.f, 0.f, 0.f};
  const int so_ml = WS_ML + (bh * NSPLIT + 4 * wave) * 128;
  u32x2 ml = {0u, 0u};
  unsigned ebound = 0;
  f32x2 fac = {0.f, 0.f};
  const unsigned head = 4 * (g & 1) + 2 * (g >> 1);
  {
    const char* v0 = (const char*)(lds + L_RING) + (16 * (g >> 1) + (g & 1)) * 512 + ((4 * wave + (i >> 2)) ^ (4 * (g & 1))) * 16 + (i & 3) * 4;
    const float* vc = lds + L_VC + 8 * g;
    const unsigned short* pr = (const unsigned short*)(lds + L_P) + (i & 7) * PROW + 8 * g;
#pragma unroll 1
    for (int hs = 0;; ++hs) {
      u32x4 pa[2]; f32x4 vs[4], vz[4];
#pragma unroll
      for (int m = 0; m < 2; ++m) {
        pa[m] = *(const u32x4*)(pr + hs * 64 + m * 32);
        vs[2 * m] = *(const f32x4*)(vc + hs * 64 + m * 32); vs[2 * m + 1] = *(const f32x4*)(vc + hs * 64 + m * 32 + 4);
        vz[2 * m] = *(const f32x4*)(vc + NSTEP * 128 + hs * 64 + m * 32); vz[2 * m + 1] = *(const f32x4*)(vc + NSTEP * 128 + hs * 64 + m * 32 + 4);
      }
#pragma unroll
      for (int m = 0; m < 2; ++m) {
        asm volatile("" : "+v"(acc));
        if (m == 0) WAIT_FULL(4 * NSTEP + 2 * hs + 1);
        else if (hs == 2 * NSTEP - 1) { __builtin_amdgcn_s_barrier(); asm volatile("" ::: "memory"); }
        unsigned x[8];
#pragma unroll
        for (int j = 0; j < 8; ++j) x[j] = *(const unsigned*)(v0 + slot + m * 16384 + j * 1024);
        if (m == 1) {
          RELEASE(2 * NSTEP + hs + 1);
          asm volatile("" : "+v"(x[0]), "+v"(x[1]), "+v"(x[2]), "+v"(x[3]), "+v"(x[4]), "+v"(x[5]), "+v"(x[6]), "+v"(x[7]) :: "memory");
        }
        u32x4 b;
#pragma unroll
        for (int e = 0; e < 4; ++e)
          b[e] = deq2(x[2 * e], x[2 * e + 1], f32x2{vz[2 * m + (e >> 1)][2 * (e & 1)], vz[2 * m + (e >> 1)][2 * (e & 1) + 1]},
                      f32x2{vs[2 * m + (e >> 1)][2 * (e & 1)], vs[2 * m + (e >> 1)][2 * (e & 1) + 1]});
        acc = __builtin_amdgcn_mfma_f32_16x16x32_bf16(__builtin_bit_cast(bf16x8, pa[m]), __builtin_bit_cast(bf16x8, b), acc, 0, 0, 0);
      }
      if (hs == 2 * NSTEP - 1) break;
      NEXT_SLOT();
      if (hs == 2 * NSTEP - 4) {
#pragma unroll 1
        for (unsigned spins = 0; __builtin_amdgcn_ballot_w64(((ml[1] ^ tag) & ~paymask) != 0u) != 0ull && spins < (1u << 20); ++spins) {
          __builtin_amdgcn_s_sleep(8);
          asm volatile("" ::: "memory");
          ml = __builtin_amdgcn_raw_buffer_load_b64(rW, lane * 8, so_ml, AUX_SC1);
        }
        *(u32x2*)(lds + L_ML + (i >> 1) * 128 + (4 * wave + g) * 4 + hf * 2) = ml;
        MEET(4);
        ml = *(const u32x2*)(lds + L_ML + wave * 128 + lane * 2);
        const float f = out_factor(__uint_as_float(ml[0]), m_own);
        ebound = umax64(ml[1] & paymask);
        if (lane == 0) lds[L_FAC + wave] = f;
        MEET(5);
        fac = *(const f32x2*)(lds + L_FAC + head);
      }
      ml = __builtin_amdgcn_raw_buffer_load_b64(rW, hs == 2 * NSTEP - 5 ? lane * 8 : OOB, so_ml, AUX_SC1);
    }
  }
  const int kexp = 151 - (int)ebound;
  const unsigned el = (bh * REP + head) * D + 16 * wave + i;
  unsigned long long* acc64 = (unsigned long long*)((char*)ws + WS_ACC) + el;
  const float o0 = (g >> 1 ? acc[2] : acc[0]) * fac[0], o1 = (g >> 1 ? acc[3] : acc[1]) * fac[1];
  const unsigned long long a0 = ((unsigned long long)(long long)__builtin_rint(__builtin_ldexp((double)o0, kexp)) << 6) + 1;
  const unsigned long long a1 = ((unsigned long long)(long long)__builtin_rint(__builtin_ldexp((double)o1, kexp)) << 6) + 1;
  const long long r0 = (long long)(__hip_atomic_fetch_add(acc64, a0, __ATOMIC_RELAXED, __HIP_MEMORY_SCOPE_AGENT) + a0);
  const long long r1 = (long long)(__hip_atomic_fetch_add(acc64 + D, a1, __ATOMIC_RELAXED, __HIP_MEMORY_SCOPE_AGENT) + a1);
  const f32x2 nanp = ebound == 255u ? f32x2{__uint_as_float(0x7fc00000u), __uint_as_float(0x7fc00000u)} : fac - fac;
  const unsigned pk = pk_bf16((float)__builtin_ldexp((double)(r0 >> 6), -kexp) + nanp[0], (float)__builtin_ldexp((double)(r1 >> 6), -kexp) + nanp[1]);
  auto rO = __builtin_amdgcn_make_buffer_rsrc((void*)out, 0, B * HQ * D * 2, RSRC);
  __builtin_amdgcn_raw_buffer_store_b16((unsigned short)pk, rO, ((unsigned)r0 & 63u) == NSPLIT ? el * 2 : OOB, 0, AUX_SC1);
  __builtin_amdgcn_raw_buffer_store_b16((unsigned short)(pk >> 16), rO, ((unsigned)r1 & 63u) == NSPLIT ? el * 2 + D * 2 : OOB, 0, AUX_SC1);
  __builtin_amdgcn_raw_buffer_store_b32(epoch + 1u, rW, (tid | split) == 0 ? 0 : OOB, bh * 256, AUX_SC1);
}

extern "C" void kernel_launch(void* const* d_in, const int* in_sizes, int n_in, void* d_out, int out_size, void* d_ws, size_t ws_size,
                              hipStream_t stream) {
  kvq_decode_kernel<<<NBH * NSPLIT, (NWAVE + 1) * 64, 0, stream>>>((const unsigned short*)d_in[0], (const int*)d_in[1], (const int*)d_in[2],
                                                                   (const unsigned short*)d_in[3], (const unsigned short*)d_in[4],
                                                                   (const unsigned short*)d_in[5], (const unsigned short*)d_in[6],
                                                                   (unsigned*)d_ws, (unsigned short*)d_out);
}
```

```cpp
#include <hip/hip_runtime.h>
#include <hip/hip_bf16.h>
#include <stdint.h>

typedef unsigned u32x4 __attribute__((ext_vector_type(4)));
typedef unsigned u32x2 __attribute__((ext_vector_type(2)));
typedef int      i32x4 __attribute__((ext_vector_type(4)));
typedef float    f32x4 __attribute__((ext_vector_type(4)));
typedef float    f32x2 __attribute__((ext_vector_type(2)));
typedef __bf16   bf16x2 __attribute__((ext_vector_type(2)));
typedef __bf16   bf16x8 __attribute__((ext_vector_type(8)));
typedef volatile __attribute__((address_space(3))) unsigned lds_word;

constexpr int AUX_SC1 = 16, RSRC = 0x00020000;
constexpr int OOB = 0x40000000;
constexpr int B = 2, HQ = 32, HKV = 4, SKV = 32768, D = 128, G = 128, NG = SKV / G, REP = HQ / HKV;
constexpr int NBH = B * HKV, NWAVE = 8, NSPLIT = 32, NSTEP = NG / NSPLIT;
constexpr int STEP_BYTES = NSPLIT * G * D * 4;
constexpr float LOG2E = 1.4426950408889634f, SCALE = 0.088388347648318440f;
constexpr int INFL = 20, INFL_LAST = 31;
static_assert(INFL < 24 && INFL_LAST < 32, "the loader publishes a slot after 24 (32) loads of the next one");
constexpr int WS_ML = 4096, WS_ACC = WS_ML + NBH * NSPLIT * REP * 16, WS_END = WS_ACC + NBH * REP * D * 8;
constexpr int NSLOT = 4, SLOT_BYTES = 32768, PROW = NSTEP * G + 8;
constexpr int L_VC = 0, L_MX = 2 * NSTEP * 128, L_SUM = L_MX + NWAVE * REP, L_AMAX = L_SUM + NWAVE * REP, L_FAC = L_AMAX + NWAVE,
              L_CTL = L_FAC + REP, L_ML = L_CTL + 16, L_KC = L_ML + REP * NSPLIT * 4, L_Q = L_KC + 2 * NSTEP * 128, L_P = L_KC,
              L_RING = L_P + REP * PROW / 2, L_END = L_RING + NSLOT * SLOT_BYTES / 4;
static_assert(L_Q + REP * D / 2 <= L_RING && L_KC % 4 == 0 && L_RING % 4 == 0 && L_END * 4 <= 160 * 1024, "LDS map");
constexpr int C_FULL = NWAVE, C_CNT = NWAVE + 1;

__device__ __forceinline__ unsigned pk_bf16(float lo, float hi) {
  f32x2 v = {lo, hi};
  return __builtin_bit_cast(unsigned, __builtin_convertvector(v, bf16x2));
}
__device__ __forceinline__ float bf_lo(unsigned u) { return __builtin_bit_cast(float, u << 16); }
__device__ __forceinline__ float bf_hi(unsigned u) { return __builtin_bit_cast(float, u & 0xffff0000u); }
__device__ __forceinline__ float rnd_bf16(float x) { return __builtin_bit_cast(float, pk_bf16(0.f, x)); }
template <int CTRL> __device__ __forceinline__ unsigned dppu(unsigned x) { return (unsigned)__builtin_amdgcn_mov_dpp((int)x, CTRL, 0xf, 0xf, true); }
template <int CTRL> __device__ __forceinline__ float dpp(float x) { return __uint_as_float(dppu<CTRL>(__float_as_uint(x))); }
constexpr int X1 = 0xB1, X2 = 0x4E, X7 = 0x141, X8 = 0x128;
__device__ __forceinline__ float xrow_max(float x) {
  auto s = __builtin_amdgcn_permlane16_swap(__float_as_uint(x), __float_as_uint(x), false, false);
  x = fmaxf(__uint_as_float(s[0]), __uint_as_float(s[1]));
  auto t = __builtin_amdgcn_permlane32_swap(__float_as_uint(x), __float_as_uint(x), false, false);
  return fmaxf(__uint_as_float(t[0]), __uint_as_float(t[1]));
}
__device__ __forceinline__ float xrow_sum(float x) {
  auto s = __builtin_amdgcn_permlane16_swap(__float_as_uint(x), __float_as_uint(x), false, false);
  x = __uint_as_float(s[0]) + __uint_as_float(s[1]);
  auto t = __builtin_amdgcn_permlane32_swap(__float_as_uint(x), __float_as_uint(x), false, false);
  return __uint_as_float(t[0]) + __uint_as_float(t[1]);
}
__device__ __forceinline__ unsigned umax64(unsigned x) {
  x = max(x, dppu<X1>(x)); x = max(x, dppu<X2>(x)); x = max(x, dppu<X7>(x)); x = max(x, dppu<X8>(x));
  auto s = __builtin_amdgcn_permlane16_swap(x, x, false, false);
  x = max(s[0], s[1]);
  auto t = __builtin_amdgcn_permlane32_swap(x, x, false, false);
  return max(t[0], t[1]);
}
__device__ __forceinline__ unsigned deq2(unsigned c0, unsigned c1, f32x2 z, f32x2 s) {
  f32x2 x = {(float)(int)c0, (float)(int)c1};
  x -= z;
  f32x2 r = {rnd_bf16(x[0]), rnd_bf16(x[1])};
  r *= s;
  return pk_bf16(r[0], r[1]);
}
__device__ __forceinline__ float out_factor(float x, float m_own) {
  const float m = dpp<0xA0>(x), l = dpp<0xF5>(x);
  float M = fmaxf(m, dpp<X2>(m)); M = fmaxf(M, dpp<X7>(M)); M = fmaxf(M, dpp<X8>(M)); M = xrow_max(M);
  float L = __builtin_amdgcn_exp2f((m - M) * LOG2E) * l;
  L += dpp<X2>(L); L += dpp<X7>(L); L += dpp<X8>(L); L = xrow_sum(L);
  return __builtin_amdgcn_exp2f((m_own - M) * LOG2E) / L;
}

__device__ __forceinline__ void loader(const int* qk, const int* qv, unsigned ring, lds_word* ctl, int group0, int so, int voff0, unsigned a_full, unsigned a_rel, unsigned bh, unsigned lane) {
  unsigned h = 0, lb = ring, rel;
  int xm = 128, vcur = voff0;
  i32x4 rs = {(int)(unsigned long long)qk, (int)((unsigned long long)qk >> 32) & 0xffff, NBH * SKV * D * 4, RSRC};
#define LD "s_waitcnt vmcnt(%c[n])\n\tbuffer_load_dwordx4 %[v], %[rs], %[so] offen nt lds\n\t"
#define INC(c) "s_addk_i32 m0, " c "\n\ts_addk_i32 %[so], " c "\n\t"
#define FOUR LD INC("0x1000") LD INC("0x1000") LD INC("0x1000") LD
#pragma unroll 1
  do {
    int cnt;
    asm volatile("s_mov_b32 m0, %[lb]\n\ts_mov_b32 %[cnt], 0\n"
                 "1:\n\t" FOUR INC("0x1000") FOUR INC("0x9400")
                 "v_xor_b32 %[v], %[xm], %[v]\n\ts_add_u32 %[cnt], %[cnt], 1\n\ts_cmp_lg_u32 %[cnt], 3\n\ts_cbranch_scc1 2f\n\t"
                 "ds_write_b32 %[af], %[fv]\n\tds_read_b32 %[rel], %[ar]\n"
                 "2:\n\ts_cmp_lt_u32 %[cnt], 4\n\ts_cbranch_scc1 1b\n\ts_waitcnt lgkmcnt(0)"
                 : [so] "+&s"(so), [rel] "=&v"(rel), [v] "+&v"(vcur), [cnt] "=&s"(cnt)
                 : [lb] "s"(lb), [xm] "s"(xm), [rs] "s"(rs), [af] "v"(a_full), [fv] "v"(2 * h), [ar] "v"(a_rel), [n] "i"(INFL)
                 : "scc", "memory");
    ++h;
    lb = ring + (h % NSLOT) * SLOT_BYTES;
    so = group0 + (((h >> 1) + bh) & 7) * STEP_BYTES + (h & 1) * SLOT_BYTES;
    if (h == 2 * NSTEP) { rs[0] = (int)(unsigned long long)qv; rs[1] = (int)((unsigned long long)qv >> 32) & 0xffff; xm = 0; }
    while (__builtin_expect(__builtin_amdgcn_ballot_w64(rel + (NSLOT - 1) < h) != 0ull, 0)) rel = ctl[lane & 7];
  } while (h != 4 * NSTEP - 1);
#pragma unroll 1
  for (int k = 0; k < 8; ++k) {
    const int first = (k >> 2) * 16384 + (k & 3) * 1024;
    int so4 = so + first;
    asm volatile("s_mov_b32 m0, %[lb]\n\t" FOUR : [so] "+&s"(so4) : [lb] "s"(lb + first), [v] "v"(voff0), [rs] "s"(rs), [n] "i"(INFL_LAST) : "scc", "memory");
  }
  ctl[C_FULL] = 8 * NSTEP - 2;
  asm volatile("s_waitcnt vmcnt(16)" ::: "memory");
  ctl[C_FULL] = 8 * NSTEP - 1;
  asm volatile("s_waitcnt vmcnt(0)" ::: "memory");
  __builtin_amdgcn_s_barrier();
}

__global__ __launch_bounds__(576, 3) void kvq_decode_kernel(const unsigned short* __restrict__ q, const int* __restrict__ qk, const int* __restrict__ qv,
                                                            const unsigned short* __restrict__ ksc, const unsigned short* __restrict__ kzr,
                                                            const unsigned short* __restrict__ vsc, const unsigned short* __restrict__ vzr,
                                                            unsigned* ws, unsigned short* __restrict__ out) {
  __shared__ __attribute__((aligned(16))) float lds[L_END];
  const unsigned lane = threadIdx.x & 63, wave9 = __builtin_amdgcn_readfirstlane(threadIdx.x >> 6);
  const unsigned bh = blockIdx.x % NBH, split = blockIdx.x / NBH;
  lds_word* ctl = (lds_word*)(lds + L_CTL);
  if (threadIdx.x < 12) ctl[lane] = 0u;
  int voff0 = (lane >> 5) * 512 + ((lane & 31) ^ (4 * (lane >> 5))) * 16, group0 = (bh * SKV + split * G) * (D * 4), so0 = group0 + bh * STEP_BYTES;
  unsigned a_full = (unsigned)(size_t)(ctl + C_FULL), a_rel = (unsigned)(size_t)(ctl + (lane & 7));
  asm volatile("" : "+v"(voff0), "+s"(group0), "+s"(so0), "+v"(a_full), "+v"(a_rel));
  asm volatile("" :: "s"(q), "s"(qk), "s"(qv), "s"(ksc), "s"(kzr), "s"(vsc), "s"(vzr), "s"(ws));
  asm volatile("s_waitcnt lgkmcnt(0)" ::: "memory");
  __builtin_amdgcn_s_barrier();
  asm volatile("" ::: "memory");
  if (__builtin_expect(wave9 == 0, 1)) {
    __builtin_amdgcn_s_setprio(3);
    loader(qk, qv, (unsigned)(size_t)(__attribute__((address_space(3))) float*)(lds + L_RING), ctl, group0, so0, voff0, a_full, a_rel, bh, lane);
    return;
  }
  const unsigned tid = threadIdx.x - 64, wave = wave9 - 1;
  const unsigned i = lane & 15, g = lane >> 4, hf = i & 1;
  auto rW = __builtin_amdgcn_make_buffer_rsrc((void*)ws, 0, WS_END, RSRC);
#define WAIT_FULL(n) do { while (__builtin_amdgcn_readfirstlane(ctl[C_FULL]) < (n)) __builtin_amdgcn_s_sleep(1); asm volatile("" ::: "memory"); } while (0)
#define RELEASE(n) do { asm volatile("s_waitcnt lgkmcnt(0)" ::: "memory"); ctl[wave] = (n); } while (0)
#define MEET(k) do { asm volatile("s_waitcnt lgkmcnt(0)" ::: "memory"); \
    if (lane == 0) __hip_atomic_fetch_add((unsigned*)(lds + L_CTL + C_CNT), 1u, __ATOMIC_RELAXED, __HIP_MEMORY_SCOPE_WORKGROUP); \
    while (__builtin_amdgcn_readfirstlane(ctl[C_CNT]) < NWAVE * (k)) __builtin_amdgcn_s_sleep(1); asm volatile("" ::: "memory"); } while (0)
  auto rO = __builtin_amdgcn_make_buffer_rsrc((void*)out, 0, B * HQ * D * 2, RSRC);
  unsigned epoch;
  {
    const unsigned arr = wave >> 1, stp = (wave & 1) * 4 + g;
    const unsigned short* src = arr == 0 ? ksc : arr == 1 ? kzr : arr == 2 ? vsc : vzr;
    const u32x4 st = __builtin_amdgcn_raw_buffer_load_b128(__builtin_amdgcn_make_buffer_rsrc((void*)src, 0, NBH * NG * 256, RSRC),
                                                           ((stp + bh) & 7) * (NSPLIT * 256) + i * 16, (bh * NG + split) * 256, 0);
    const unsigned qw = __builtin_amdgcn_raw_buffer_load_b32(__builtin_amdgcn_make_buffer_rsrc((void*)q, 0, B * HQ * D * 2, RSRC), tid * 4, bh * (REP * D * 2), 0);
    epoch = __builtin_amdgcn_raw_buffer_load_b32(rW, 0, bh * 256, AUX_SC1);
    __builtin_amdgcn_raw_buffer_store_b128(u32x4{0u, 0u, 0u, 0u}, rW, tid < 16 ? lane * 16 : OOB, WS_ACC + (bh * NSPLIT + split) * 256, AUX_SC1);
    float* dst = lds + (arr < 2 ? L_KC : L_VC) + ((arr & 1) * NSTEP + stp) * 128;
    if (arr < 2) {
      *(f32x4*)(dst + i * 8) = f32x4{bf_lo(st[0]), bf_hi(st[0]), bf_lo(st[1]), bf_hi(st[1])};
      *(f32x4*)(dst + i * 8 + 4) = f32x4{bf_lo(st[2]), bf_hi(st[2]), bf_lo(st[3]), bf_hi(st[3])};
    } else {
      *(f32x4*)(dst + (i >> 1) * 16 + hf * 4) = f32x4{bf_lo(st[0]), bf_lo(st[1]), bf_lo(st[2]), bf_lo(st[3])};
      *(f32x4*)(dst + (i >> 1) * 16 + hf * 4 + 8) = f32x4{bf_hi(st[0]), bf_hi(st[1]), bf_hi(st[2]), bf_hi(st[3])};
    }
    lds[L_Q + tid] = __uint_as_float(qw);
    unsigned am = 0;
#pragma unroll
    for (int k = 0; k < 4; ++k) am = max(am, max((st[k] << 16) & 0x7fff0000u, st[k] & 0x7fff0000u));
    am = umax64(am);
    if (lane == 0) lds[L_AMAX + wave] = __uint_as_float(am);
  }
  unsigned slot = 0;
#define NEXT_SLOT() (slot = slot == (NSLOT - 1) * SLOT_BYTES ? 0 : slot + SLOT_BYTES)
  float s[16];
#pragma unroll
  for (int T = 0; T < 16; ++T) s[T] = 0.f;
  {
    u32x4 qb[2] = {{0u, 0u, 0u, 0u}, {0u, 0u, 0u, 0u}};
    const unsigned tt = i >> 1, ch = (2 * g + hf) ^ (4 * (tt & 3));
    const char* k0 = (const char*)(lds + L_RING) + (wave * 8 + tt) * 512 + ch * 16;
    const char* k1 = (const char*)(lds + L_RING) + (wave * 8 + tt) * 512 + (ch ^ 8) * 16;
#pragma unroll 1
    for (int it = 0; it < NSTEP; ++it) {
      const float* kc = lds + L_KC + it * 128 + (2 * g + hf) * 4;
#pragma unroll
      for (int T = 0; T < 14; ++T) s[T] = s[T + 2];
      f32x4 ks[4], kz[4];
#pragma unroll
      for (int u = 0; u < 2; ++u) {
        WAIT_FULL(4 * it + 2 * u + 2);
        u32x4 x[4];
#pragma unroll
        for (int p = 0; p < 4; ++p) x[p] = *(const u32x4*)(((p & 1) ? k1 : k0) + slot + (p >> 1) * 256);
        RELEASE(2 * it + u + 1);
        asm volatile("" : "+v"(x[0]), "+v"(x[1]), "+v"(x[2]), "+v"(x[3]) :: "memory");
        NEXT_SLOT();
        if (u == 0) {
          if (it == 0) {
            MEET(1);
            const u32x2* qp = (const u32x2*)(lds + L_Q + (i >> 1) * (D / 2) + (2 * g + hf) * 2);
            const u32x2 q0 = qp[0], q1 = qp[8], q2 = qp[16], q3 = qp[24];
            qb[0] = u32x4{q0[0], q0[1], q1[0], q1[1]}; qb[1] = u32x4{q2[0], q2[1], q3[0], q3[1]};
          }
#pragma unroll
          for (int p = 0; p < 4; ++p) { ks[p] = *(const f32x4*)(kc + p * 32); kz[p] = *(const f32x4*)(kc + NSTEP * 128 + p * 32); }
        }
        f32x4 d = {0.f, 0.f, 0.f, 0.f};
#pragma unroll
        for (int m = 0; m < 2; ++m) {
          u32x4 a;
#pragma unroll
          for (int e = 0; e < 2; ++e) {
            const int p = 2 * m + e;
            a[2 * e + 0] = deq2(x[p][0], x[p][1], f32x2{kz[p][0], kz[p][1]}, f32x2{ks[p][0], ks[p][1]});
            a[2 * e + 1] = deq2(x[p][2], x[p][3], f32x2{kz[p][2], kz[p][3]}, f32x2{ks[p][2], ks[p][3]});
          }
          d = __builtin_amdgcn_mfma_f32_16x16x32_bf16(__builtin_bit_cast(bf16x8, a), __builtin_bit_cast(bf16x8, qb[m]), d, 0, 0, 0);
        }
        const float mine = hf ? d[3] : d[0], send = hf ? d[1] : d[2];
        s[14 + u] = rnd_bf16(rnd_bf16(mine + dpp<X1>(send)) * SCALE);
        asm volatile("" : "+v"(s[14 + u]));
      }
    }
  }
  float mx = s[0];
#pragma unroll
  for (int T = 1; T < 16; ++T) mx = fmaxf(mx, s[T]);
  mx = fmaxf(mx, dpp<X1>(mx));
  mx = xrow_max(mx);
  if (lane < 16 && hf == 0) lds[L_MX + (i >> 1) * NWAVE + wave] = mx;
  MEET(2);
  {
    const f32x4 m0 = *(const f32x4*)(lds + L_MX + (i >> 1) * NWAVE), m1 = *(const f32x4*)(lds + L_MX + (i >> 1) * NWAVE + 4);
    mx = fmaxf(fmaxf(fmaxf(m0[0], m0[1]), fmaxf(m0[2], m0[3])), fmaxf(fmaxf(m1[0], m1[1]), fmaxf(m1[2], m1[3])));
  }
  float lsum = 0.f;
  {
    unsigned short* pw = (unsigned short*)(lds + L_P) + (i >> 1) * PROW + (wave >> 1) * 16 + hf * 8 + (wave & 1) * 4 + g;
#pragma unroll
    for (int T = 0; T < 16; ++T) {
      const float p = __builtin_amdgcn_exp2f((s[T] - mx) * LOG2E);
      lsum += p;
      pw[T * 64] = (unsigned short)pk_bf16(p, p);
    }
  }
  lsum += dpp<X1>(lsum);
  lsum = xrow_sum(lsum);
  if (lane < 16 && hf == 0) lds[L_SUM + (i >> 1) * NWAVE + wave] = lsum;
  const unsigned tag = ((epoch + 1u) * 0x9E3779B1u) ^ (0u - hf), paymask = hf ? 0xffu : 0u;
  const float m_own = __int_as_float(__builtin_amdgcn_readlane(__float_as_int(mx), 2 * wave));
  asm volatile("s_waitcnt vmcnt(0)" ::: "memory");
  MEET(3);
  if (wave == 0) {
    const f32x4 l0 = *(const f32x4*)(lds + L_SUM + (i >> 1) * NWAVE), l1 = *(const f32x4*)(lds + L_SUM + (i >> 1) * NWAVE + 4);
    const u32x4 am = *(const u32x4*)(lds + L_AMAX + 4);
    const float L = ((l0[0] + l0[1]) + (l0[2] + l0[3])) + ((l1[0] + l1[1]) + (l1[2] + l1[3]));
    const float vb = (1.f + __uint_as_float(max(am[2], am[3])) * 0x1p-31f) * __uint_as_float(max(am[0], am[1]));
    __builtin_amdgcn_raw_buffer_store_b64(u32x2{__float_as_uint(hf ? L : mx), (tag & ~paymask) | ((__float_as_uint(vb) >> 23) & paymask)}, rW,
                                          lane < 16 ? i * 8 : OOB, WS_ML + (bh * NSPLIT + split) * 128, AUX_SC1);
  }
  f32x4 acc = {0.f, 0.f, 0.f, 0.f};
  const int so_ml = WS_ML + (bh * NSPLIT + 4 * wave) * 128;
  u32x2 ml = {0u, 0u};
  unsigned ebound = 0;
  f32x2 fac = {0.f, 0.f};
  const unsigned head = 4 * (g & 1) + 2 * (g >> 1);
  {
    const char* v0 = (const char*)(lds + L_RING) + (16 * (g >> 1) + (g & 1)) * 512 + ((4 * wave + (i >> 2)) ^ (4 * (g & 1))) * 16 + (i & 3) * 4;
    const float* vc = lds + L_VC + 8 * g;
    const unsigned short* pr = (const unsigned short*)(lds + L_P) + (i & 7) * PROW + 8 * g;
#pragma unroll 1
    for (int hs = 0;; ++hs) {
      u32x4 pa[2]; f32x4 vs[4], vz[4];
#pragma unroll
      for (int m = 0; m < 2; ++m) {
        pa[m] = *(const u32x4*)(pr + hs * 64 + m * 32);
        vs[2 * m] = *(const f32x4*)(vc + hs * 64 + m * 32); vs[2 * m + 1] = *(const f32x4*)(vc + hs * 64 + m * 32 + 4);
        vz[2 * m] = *(const f32x4*)(vc + NSTEP * 128 + hs * 64 + m * 32); vz[2 * m + 1] = *(const f32x4*)(vc + NSTEP * 128 + hs * 64 + m * 32 + 4);
      }
#pragma unroll
      for (int m = 0; m < 2; ++m) {
        asm volatile("" : "+v"(acc));
        if (m == 0) WAIT_FULL(4 * NSTEP + 2 * hs + 1);
        else if (hs == 2 * NSTEP - 1) { __builtin_amdgcn_s_barrier(); asm volatile("" ::: "memory"); }
        unsigned x[8];
#pragma unroll
        for (int j = 0; j < 8; ++j) x[j] = *(const unsigned*)(v0 + slot + m * 16384 + j * 1024);
        if (m == 1) {
          RELEASE(2 * NSTEP + hs + 1);
          asm volatile("" : "+v"(x[0]), "+v"(x[1]), "+v"(x[2]), "+v"(x[3]), "+v"(x[4]), "+v"(x[5]), "+v"(x[6]), "+v"(x[7]) :: "memory");
        }
        u32x4 b;
#pragma unroll
        for (int e = 0; e < 4; ++e)
          b[e] = deq2(x[2 * e], x[2 * e + 1], f32x2{vz[2 * m + (e >> 1)][2 * (e & 1)], vz[2 * m + (e >> 1)][2 * (e & 1) + 1]},
                      f32x2{vs[2 * m + (e >> 1)][2 * (e & 1)], vs[2 * m + (e >> 1)][2 * (e & 1) + 1]});
        acc = __builtin_amdgcn_mfma_f32_16x16x32_bf16(__builtin_bit_cast(bf16x8, pa[m]), __builtin_bit_cast(bf16x8, b), acc, 0, 0, 0);
      }
      if (hs == 2 * NSTEP - 1) break;
      NEXT_SLOT();
      if (hs == 2 * NSTEP - 4) {
#pragma unroll 1
        for (unsigned spins = 0; __builtin_amdgcn_ballot_w64(((ml[1] ^ tag) & ~paymask) != 0u) != 0ull && spins < (1u << 20); ++spins) {
          __builtin_amdgcn_s_sleep(8);
          asm volatile("" ::: "memory");
          ml = __builtin_amdgcn_raw_buffer_load_b64(rW, lane * 8, so_ml, AUX_SC1);
        }
        *(u32x2*)(lds + L_ML + (i >> 1) * 128 + (4 * wave + g) * 4 + hf * 2) = ml;
        MEET(4);
        ml = *(const u32x2*)(lds + L_ML + wave * 128 + lane * 2);
        const float f = out_factor(__uint_as_float(ml[0]), m_own);
        ebound = umax64(ml[1] & paymask);
        if (lane == 0) lds[L_FAC + wave] = f;
        MEET(5);
        __builtin_amdgcn_raw_buffer_store_b32(epoch + 1u, rW, (tid | split) == 0 ? 0 : OOB, bh * 256, AUX_SC1);
        fac = *(const f32x2*)(lds + L_FAC + head);
      }
      ml = __builtin_amdgcn_raw_buffer_load_b64(rW, hs == 2 * NSTEP - 5 ? lane * 8 : OOB, so_ml, AUX_SC1);
    }
  }
  const int kexp = 151 - (int)ebound;
  const unsigned el = (bh * REP + head) * D + 16 * wave + i;
  unsigned long long* acc64 = (unsigned long long*)((char*)ws + WS_ACC) + el;
  const float o0 = (g >> 1 ? acc[2] : acc[0]) * fac[0], o1 = (g >> 1 ? acc[3] : acc[1]) * fac[1];
  const unsigned long long a0 = ((unsigned long long)(long long)__builtin_rint(__builtin_ldexp((double)o0, kexp)) << 6) + 1;
  const unsigned long long a1 = ((unsigned long long)(long long)__builtin_rint(__builtin_ldexp((double)o1, kexp)) << 6) + 1;
  const long long r0 = (long long)(__hip_atomic_fetch_add(acc64, a0, __ATOMIC_RELAXED, __HIP_MEMORY_SCOPE_AGENT) + a0);
  const long long r1 = (long long)(__hip_atomic_fetch_add(acc64 + D, a1, __ATOMIC_RELAXED, __HIP_MEMORY_SCOPE_AGENT) + a1);
  const f32x2 nanp = ebound == 255u ? f32x2{__uint_as_float(0x7fc00000u), __uint_as_float(0x7fc00000u)} : fac - fac;
  const unsigned pk = pk_bf16((float)__builtin_ldexp((double)(r0 >> 6), -kexp) + nanp[0], (float)__builtin_ldexp((double)(r1 >> 6), -kexp) + nanp[1]);
  __builtin_amdgcn_raw_buffer_store_b16((unsigned short)pk, rO, ((unsigned)r0 & 63u) == NSPLIT ? el * 2 : OOB, 0, 0);
  __builtin_amdgcn_raw_buffer_store_b16((unsigned short)(pk >> 16), rO, ((unsigned)r1 & 63u) == NSPLIT ? el * 2 + D * 2 : OOB, 0, 0);
}

extern "C" void kernel_launch(void* const* d_in, const int* in_sizes, int n_in, void* d_out, int out_size, void* d_ws, size_t ws_size,
                              hipStream_t stream) {
  kvq_decode_kernel<<<NBH * NSPLIT, (NWAVE + 1) * 64, 0, stream>>>((const unsigned short*)d_in[0], (const int*)d_in[1], (const int*)d_in[2],
                                                                   (const unsigned short*)d_in[3], (const unsigned short*)d_in[4],
                                                                   (const unsigned short*)d_in[5], (const unsigned short*)d_in[6],
                                                                   (unsigned*)d_ws, (unsigned short*)d_out);
}
```

```cpp
#include <hip/hip_runtime.h>
#include <hip/hip_bf16.h>
#include <stdint.h>

typedef unsigned u32x4 __attribute__((ext_vector_type(4)));
typedef unsigned u32x2 __attribute__((ext_vector_type(2)));
typedef int      i32x4 __attribute__((ext_vector_type(4)));
typedef float    f32x4 __attribute__((ext_vector_type(4)));
typedef float    f32x2 __attribute__((ext_vector_type(2)));
typedef __bf16   bf16x2 __attribute__((ext_vector_type(2)));
typedef __bf16   bf16x8 __attribute__((ext_vector_type(8)));
typedef volatile __attribute__((address_space(3))) unsigned lds_word;

constexpr int AUX_SC1 = 16, RSRC = 0x00020000;
constexpr int OOB = 0x40000000;
constexpr int B = 2, HQ = 32, HKV = 4, SKV = 32768, D = 128, G = 128, NG = SKV / G, REP = HQ / HKV;
constexpr int NBH = B * HKV, NWAVE = 8, NSPLIT = 32, NSTEP = NG / NSPLIT;
constexpr int STEP_BYTES = NSPLIT * G * D * 4;
constexpr float LOG2E = 1.4426950408889634f, SCALE = 0.088388347648318440f;
constexpr int INFL = 20, INFL_LAST = 31;
static_assert(INFL + 1 < 24 && INFL_LAST < 32, "the loader publishes a slot after 24 (32) loads of the next one");
constexpr int GATHER = 10;
constexpr int WS_ML = 4096, WS_ACC = WS_ML + NBH * NSPLIT * REP * 16, WS_END = WS_ACC + NBH * REP * D * 8;
constexpr int NSLOT = 4, SLOT_BYTES = 32768, PROW = NSTEP * G + 8;
constexpr int L_VC = 0, L_MX = 2 * NSTEP * 128, L_SUM = L_MX + NWAVE * REP, L_AMAX = L_SUM + NWAVE * REP, L_FAC = L_AMAX + NWAVE,
              L_CTL = L_FAC + REP, L_ML = L_CTL + 16, L_KC = L_ML + REP * NSPLIT * 4, L_Q = L_KC + 2 * NSTEP * 128, L_P = L_KC,
              L_RING = L_P + REP * PROW / 2, L_END = L_RING + NSLOT * SLOT_BYTES / 4;
static_assert(L_Q + REP * D / 2 <= L_RING && L_KC % 4 == 0 && L_RING % 4 == 0 && L_END * 4 <= 160 * 1024, "LDS map");
constexpr int C_FULL = NWAVE, C_CNT = NWAVE + 1;

__device__ __forceinline__ unsigned pk_bf16(float lo, float hi) {
  f32x2 v = {lo, hi};
  return __builtin_bit_cast(unsigned, __builtin_convertvector(v, bf16x2));
}
__device__ __forceinline__ float bf_lo(unsigned u) { return __builtin_bit_cast(float, u << 16); }
__device__ __forceinline__ float bf_hi(unsigned u) { return __builtin_bit_cast(float, u & 0xffff0000u); }
__device__ __forceinline__ float rnd_bf16(float x) { return __builtin_bit_cast(float, pk_bf16(0.f, x)); }
template <int CTRL> __device__ __forceinline__ unsigned dppu(unsigned x) { return (unsigned)__builtin_amdgcn_mov_dpp((int)x, CTRL, 0xf, 0xf, true); }
template <int CTRL> __device__ __forceinline__ float dpp(float x) { return __uint_as_float(dppu<CTRL>(__float_as_uint(x))); }
constexpr int X1 = 0xB1, X2 = 0x4E, X7 = 0x141, X8 = 0x128;
__device__ __forceinline__ float xrow_max(float x) {
  auto s = __builtin_amdgcn_permlane16_swap(__float_as_uint(x), __float_as_uint(x), false, false);
  x = fmaxf(__uint_as_float(s[0]), __uint_as_float(s[1]));
  auto t = __builtin_amdgcn_permlane32_swap(__float_as_uint(x), __float_as_uint(x), false, false);
  return fmaxf(__uint_as_float(t[0]), __uint_as_float(t[1]));
}
__device__ __forceinline__ float xrow_sum(float x) {
  auto s = __builtin_amdgcn_permlane16_swap(__float_as_uint(x), __float_as_uint(x), false, false);
  x = __uint_as_float(s[0]) + __uint_as_float(s[1]);
  auto t = __builtin_amdgcn_permlane32_swap(__float_as_uint(x), __float_as_uint(x), false, false);
  return __uint_as_float(t[0]) + __uint_as_float(t[1]);
}
__device__ __forceinline__ unsigned umax64(unsigned x) {
  x = max(x, dppu<X1>(x)); x = max(x, dppu<X2>(x)); x = max(x, dppu<X7>(x)); x = max(x, dppu<X8>(x));
  auto s = __builtin_amdgcn_permlane16_swap(x, x, false, false);
  x = max(s[0], s[1]);
  auto t = __builtin_amdgcn_permlane32_swap(x, x, false, false);
  return max(t[0], t[1]);
}
__device__ __forceinline__ unsigned deq2(unsigned c0, unsigned c1, f32x2 z, f32x2 s) {
  f32x2 x = {(float)(int)c0, (float)(int)c1};
  x -= z;
  f32x2 r = {rnd_bf16(x[0]), rnd_bf16(x[1])};
  r *= s;
  return pk_bf16(r[0], r[1]);
}
__device__ __forceinline__ float out_factor(float x, float m_own) {
  const float m = dpp<0xA0>(x), l = dpp<0xF5>(x);
  float M = fmaxf(m, dpp<X2>(m)); M = fmaxf(M, dpp<X7>(M)); M = fmaxf(M, dpp<X8>(M)); M = xrow_max(M);
  float L = __builtin_amdgcn_exp2f((m - M) * LOG2E) * l;
  L += dpp<X2>(L); L += dpp<X7>(L); L += dpp<X8>(L); L = xrow_sum(L);
  return __builtin_amdgcn_exp2f((m_own - M) * LOG2E) / L;
}

__device__ __forceinline__ void loader(const int* qk, const int* qv, unsigned ring, lds_word* ctl, int group0, int so, int voff0, unsigned a_full, unsigned a_rel, unsigned bh, unsigned lane) {
  unsigned h = 0, lb = ring, rel;
  int xm = 128, vcur = voff0;
  i32x4 rs = {(int)(unsigned long long)qk, (int)((unsigned long long)qk >> 32) & 0xffff, NBH * SKV * D * 4, RSRC};
#define LD(n) "s_waitcnt vmcnt(%c[" n "])\n\tbuffer_load_dwordx4 %[v], %[rs], %[so] offen nt lds\n\t"
#define INC(c) "s_addk_i32 m0, " c "\n\ts_addk_i32 %[so], " c "\n\t"
#define FOUR(n) LD(n) INC("0x1000") LD(n) INC("0x1000") LD(n) INC("0x1000") LD(n)
#pragma unroll 1
  do {
    int cnt;
    asm volatile("s_mov_b32 m0, %[lb]\n\ts_mov_b32 %[cnt], 0\n"
                 "1:\n\t" FOUR("n0") INC("0x1000") FOUR("n1") INC("0x9400")
                 "v_xor_b32 %[v], %[xm], %[v]\n\ts_add_u32 %[cnt], %[cnt], 1\n\ts_cmp_lg_u32 %[cnt], 3\n\ts_cbranch_scc1 2f\n\t"
                 "ds_write_b32 %[af], %[fv]\n\tds_read_b32 %[rel], %[ar]\n"
                 "2:\n\ts_cmp_lt_u32 %[cnt], 4\n\ts_cbranch_scc1 1b\n\ts_waitcnt lgkmcnt(0)"
                 : [so] "+&s"(so), [rel] "=&v"(rel), [v] "+&v"(vcur), [cnt] "=&s"(cnt)
                 : [lb] "s"(lb), [xm] "s"(xm), [rs] "s"(rs), [af] "v"(a_full), [fv] "v"(2 * h), [ar] "v"(a_rel), [n0] "i"(INFL), [n1] "i"(INFL + 1)
                 : "scc", "memory");
    ++h;
    lb = ring + (h % NSLOT) * SLOT_BYTES;
    so = group0 + (((h >> 1) + bh) & 7) * STEP_BYTES + (h & 1) * SLOT_BYTES;
    if (h == 2 * NSTEP) { rs[0] = (int)(unsigned long long)qv; rs[1] = (int)((unsigned long long)qv >> 32) & 0xffff; xm = 0; }
    while (__builtin_expect(__builtin_amdgcn_ballot_w64(rel + (NSLOT - 1) < h) != 0ull, 0)) rel = ctl[lane & 7];
  } while (h != 4 * NSTEP - 1);
#pragma unroll 1
  for (int k = 0; k < 8; ++k) {
    const int first = (k >> 2) * 16384 + (k & 3) * 1024;
    int so4 = so + first;
    asm volatile("s_mov_b32 m0, %[lb]\n\t" FOUR("n") : [so] "+&s"(so4) : [lb] "s"(lb + first), [v] "v"(voff0), [rs] "s"(rs), [n] "i"(INFL_LAST) : "scc", "memory");
  }
  ctl[C_FULL] = 8 * NSTEP - 2;
  asm volatile("s_waitcnt vmcnt(16)" ::: "memory");
  ctl[C_FULL] = 8 * NSTEP - 1;
  asm volatile("s_waitcnt vmcnt(0)" ::: "memory");
  __builtin_amdgcn_s_barrier();
}

__global__ __launch_bounds__(576, 3) void kvq_decode_kernel(const unsigned short* __restrict__ q, const int* __restrict__ qk, const int* __restrict__ qv,
                                                            const unsigned short* __restrict__ ksc, const unsigned short* __restrict__ kzr,
                                                            const unsigned short* __restrict__ vsc, const unsigned short* __restrict__ vzr,
                                                            unsigned* ws, unsigned short* __restrict__ out) {
  __shared__ __attribute__((aligned(16))) float lds[L_END];
  const unsigned lane = threadIdx.x & 63, wave9 = __builtin_amdgcn_readfirstlane(threadIdx.x >> 6);
  const unsigned bh = blockIdx.x % NBH, split = blockIdx.x / NBH;
  lds_word* ctl = (lds_word*)(lds + L_CTL);
  if (threadIdx.x < 12) ctl[lane] = 0u;
  int voff0 = (lane >> 5) * 512 + ((lane & 31) ^ (4 * (lane >> 5))) * 16, group0 = (bh * SKV + split * G) * (D * 4), so0 = group0 + bh * STEP_BYTES;
  unsigned a_full = (unsigned)(size_t)(ctl + C_FULL), a_rel = (unsigned)(size_t)(ctl + (lane & 7));
  asm volatile("" : "+v"(voff0), "+s"(group0), "+s"(so0), "+v"(a_full), "+v"(a_rel));
  asm volatile("" :: "s"(q), "s"(qk), "s"(qv), "s"(ksc), "s"(kzr), "s"(vsc), "s"(vzr), "s"(ws));
  asm volatile("s_waitcnt lgkmcnt(0)" ::: "memory");
  __builtin_amdgcn_s_barrier();
  asm volatile("" ::: "memory");
  if (__builtin_expect(wave9 == 0, 1)) {
    __builtin_amdgcn_s_setprio(3);
    loader(qk, qv, (unsigned)(size_t)(__attribute__((address_space(3))) float*)(lds + L_RING), ctl, group0, so0, voff0, a_full, a_rel, bh, lane);
    return;
  }
  const unsigned tid = threadIdx.x - 64, wave = wave9 - 1;
  const unsigned i = lane & 15, g = lane >> 4, hf = i & 1;
  auto rW = __builtin_amdgcn_make_buffer_rsrc((void*)ws, 0, WS_END, RSRC);
#define WAIT_FULL(n) do { while (__builtin_amdgcn_readfirstlane(ctl[C_FULL]) < (n)) __builtin_amdgcn_s_sleep(1); asm volatile("" ::: "memory"); } while (0)
#define RELEASE(n) do { asm volatile("s_waitcnt lgkmcnt(0)" ::: "memory"); ctl[wave] = (n); } while (0)
#define MEET(k) do { asm volatile("s_waitcnt lgkmcnt(0)" ::: "memory"); \
    if (lane == 0) __hip_atomic_fetch_add((unsigned*)(lds + L_CTL + C_CNT), 1u, __ATOMIC_RELAXED, __HIP_MEMORY_SCOPE_WORKGROUP); \
    while (__builtin_amdgcn_readfirstlane(ctl[C_CNT]) < NWAVE * (k)) __builtin_amdgcn_s_sleep(1); asm volatile("" ::: "memory"); } while (0)
  auto rO = __builtin_amdgcn_make_buffer_rsrc((void*)out, 0, B * HQ * D * 2, RSRC);
  unsigned epoch;
  {
    const unsigned arr = wave >> 1, stp = (wave & 1) * 4 + g;
    const unsigned short* src = arr == 0 ? ksc : arr == 1 ? kzr : arr == 2 ? vsc : vzr;
    const u32x4 st = __builtin_amdgcn_raw_buffer_load_b128(__builtin_amdgcn_make_buffer_rsrc((void*)src, 0, NBH * NG * 256, RSRC),
                                                           ((stp + bh) & 7) * (NSPLIT * 256) + i * 16, (bh * NG + split) * 256, 0);
    const unsigned qw = __builtin_amdgcn_raw_buffer_load_b32(__builtin_amdgcn_make_buffer_rsrc((void*)q, 0, B * HQ * D * 2, RSRC), tid * 4, bh * (REP * D * 2), 0);
    epoch = __builtin_amdgcn_raw_buffer_load_b32(rW, 0, bh * 256, AUX_SC1);
    __builtin_amdgcn_raw_buffer_store_b128(u32x4{0u, 0u, 0u, 0u}, rW, tid < 16 ? lane * 16 : OOB, WS_ACC + (bh * NSPLIT + split) * 256, AUX_SC1);
    float* dst = lds + (arr < 2 ? L_KC : L_VC) + ((arr & 1) * NSTEP + stp) * 128;
    if (arr < 2) {
      *(f32x4*)(dst + i * 8) = f32x4{bf_lo(st[0]), bf_hi(st[0]), bf_lo(st[1]), bf_hi(st[1])};
      *(f32x4*)(dst + i * 8 + 4) = f32x4{bf_lo(st[2]), bf_hi(st[2]), bf_lo(st[3]), bf_hi(st[3])};
    } else {
      *(f32x4*)(dst + (i >> 1) * 16 + hf * 4) = f32x4{bf_lo(st[0]), bf_lo(st[1]), bf_lo(st[2]), bf_lo(st[3])};
      *(f32x4*)(dst + (i >> 1) * 16 + hf * 4 + 8) = f32x4{bf_hi(st[0]), bf_hi(st[1]), bf_hi(st[2]), bf_hi(st[3])};
    }
    lds[L_Q + tid] = __uint_as_float(qw);
    unsigned am = 0;
#pragma unroll
    for (int k = 0; k < 4; ++k) am = max(am, max((st[k] << 16) & 0x7fff0000u, st[k] & 0x7fff0000u));
    am = umax64(am);
    if (lane == 0) lds[L_AMAX + wave] = __uint_as_float(am);
  }
  unsigned slot = 0;
#define NEXT_SLOT() (slot = slot == (NSLOT - 1) * SLOT_BYTES ? 0 : slot + SLOT_BYTES)
  float s[16];
#pragma unroll
  for (int T = 0; T < 16; ++T) s[T] = 0.f;
  {
    u32x4 qb[2] = {{0u, 0u, 0u, 0u}, {0u, 0u, 0u, 0u}};
    const unsigned tt = i >> 1, ch = (2 * g + hf) ^ (4 * (tt & 3));
    const char* k0 = (const char*)(lds + L_RING) + (wave * 8 + tt) * 512 + ch * 16;
    const char* k1 = (const char*)(lds + L_RING) + (wave * 8 + tt) * 512 + (ch ^ 8) * 16;
#pragma unroll 1
    for (int it = 0; it < NSTEP; ++it) {
      const float* kc = lds + L_KC + it * 128 + (2 * g + hf) * 4;
#pragma unroll
      for (int T = 0; T < 14; ++T) s[T] = s[T + 2];
      f32x4 ks[4], kz[4];
#pragma unroll
      for (int u = 0; u < 2; ++u) {
        WAIT_FULL(4 * it + 2 * u + 2);
        u32x4 x[4];
#pragma unroll
        for (int p = 0; p < 4; ++p) x[p] = *(const u32x4*)(((p & 1) ? k1 : k0) + slot + (p >> 1) * 256);
        RELEASE(2 * it + u + 1);
        asm volatile("" : "+v"(x[0]), "+v"(x[1]), "+v"(x[2]), "+v"(x[3]) :: "memory");
        NEXT_SLOT();
        if (u == 0) {
          if (it == 0) {
            MEET(1);
            const u32x2* qp = (const u32x2*)(lds + L_Q + (i >> 1) * (D / 2) + (2 * g + hf) * 2);
            const u32x2 q0 = qp[0], q1 = qp[8], q2 = qp[16], q3 = qp[24];
            qb[0] = u32x4{q0[0], q0[1], q1[0], q1[1]}; qb[1] = u32x4{q2[0], q2[1], q3[0], q3[1]};
          }
#pragma unroll
          for (int p = 0; p < 4; ++p) { ks[p] = *(const f32x4*)(kc + p * 32); kz[p] = *(const f32x4*)(kc + NSTEP * 128 + p * 32); }
        }
        f32x4 d = {0.f, 0.f, 0.f, 0.f};
#pragma unroll
        for (int m = 0; m < 2; ++m) {
          u32x4 a;
#pragma unroll
          for (int e = 0; e < 2; ++e) {
            const int p = 2 * m + e;
            a[2 * e + 0] = deq2(x[p][0], x[p][1], f32x2{kz[p][0], kz[p][1]}, f32x2{ks[p][0], ks[p][1]});
            a[2 * e + 1] = deq2(x[p][2], x[p][3], f32x2{kz[p][2], kz[p][3]}, f32x2{ks[p][2], ks[p][3]});
          }
          d = __builtin_amdgcn_mfma_f32_16x16x32_bf16(__builtin_bit_cast(bf16x8, a), __builtin_bit_cast(bf16x8, qb[m]), d, 0, 0, 0);
        }
        const float mine = hf ? d[3] : d[0], send = hf ? d[1] : d[2];
        s[14 + u] = rnd_bf16(rnd_bf16(mine + dpp<X1>(send)) * SCALE);
        asm volatile("" : "+v"(s[14 + u]));
      }
    }
  }
  float mx = s[0];
#pragma unroll
  for (int T = 1; T < 16; ++T) mx = fmaxf(mx, s[T]);
  mx = fmaxf(mx, dpp<X1>(mx));
  mx = xrow_max(mx);
  if (lane < 16 && hf == 0) lds[L_MX + (i >> 1) * NWAVE + wave] = mx;
  MEET(2);
  {
    const f32x4 m0 = *(const f32x4*)(lds + L_MX + (i >> 1) * NWAVE), m1 = *(const f32x4*)(lds + L_MX + (i >> 1) * NWAVE + 4);
    mx = fmaxf(fmaxf(fmaxf(m0[0], m0[1]), fmaxf(m0[2], m0[3])), fmaxf(fmaxf(m1[0], m1[1]), fmaxf(m1[2], m1[3])));
  }
  float lsum = 0.f;
  {
    unsigned short* pw = (unsigned short*)(lds + L_P) + (i >> 1) * PROW + (wave >> 1) * 16 + hf * 8 + (wave & 1) * 4 + g;
#pragma unroll
    for (int T = 0; T < 16; ++T) {
      const float p = __builtin_amdgcn_exp2f((s[T] - mx) * LOG2E);
      lsum += p;
      pw[T * 64] = (unsigned short)pk_bf16(p, p);
    }
  }
  lsum += dpp<X1>(lsum);
  lsum = xrow_sum(lsum);
  if (lane < 16 && hf == 0) lds[L_SUM + (i >> 1) * NWAVE + wave] = lsum;
  const unsigned tag = ((epoch + 1u) * 0x9E3779B1u) ^ (0u - hf), paymask = hf ? 0xffu : 0u;
  const float m_own = __int_as_float(__builtin_amdgcn_readlane(__float_as_int(mx), 2 * wave));
  asm volatile("s_waitcnt vmcnt(0)" ::: "memory");
  MEET(3);
  if (wave == 0) {
    const f32x4 l0 = *(const f32x4*)(lds + L_SUM + (i >> 1) * NWAVE), l1 = *(const f32x4*)(lds + L_SUM + (i >> 1) * NWAVE + 4);
    const u32x4 am = *(const u32x4*)(lds + L_AMAX + 4);
    const float L = ((l0[0] + l0[1]) + (l0[2] + l0[3])) + ((l1[0] + l1[1]) + (l1[2] + l1[3]));
    const float vb = (1.f + __uint_as_float(max(am[2], am[3])) * 0x1p-31f) * __uint_as_float(max(am[0], am[1]));
    __builtin_amdgcn_raw_buffer_store_b64(u32x2{__float_as_uint(hf ? L : mx), (tag & ~paymask) | ((__float_as_uint(vb) >> 23) & paymask)}, rW,
                                          lane < 16 ? i * 8 : OOB, WS_ML + (bh * NSPLIT + split) * 128, AUX_SC1);
  }
  f32x4 acc = {0.f, 0.f, 0.f, 0.f};
  const int so_ml = WS_ML + (bh * NSPLIT + 4 * wave) * 128;
  u32x2 ml = {0u, 0u};
  unsigned ebound = 0;
  f32x2 fac = {0.f, 0.f};
  const unsigned head = 4 * (g & 1) + 2 * (g >> 1);
  {
    const char* v0 = (const char*)(lds + L_RING) + (16 * (g >> 1) + (g & 1)) * 512 + ((4 * wave + (i >> 2)) ^ (4 * (g & 1))) * 16 + (i & 3) * 4;
    const float* vc = lds + L_VC + 8 * g;
    const unsigned short* pr = (const unsigned short*)(lds + L_P) + (i & 7) * PROW + 8 * g;
#pragma unroll 1
    for (int hs = 0;; ++hs) {
      u32x4 pa[2]; f32x4 vs[4], vz[4];
#pragma unroll
      for (int m = 0; m < 2; ++m) {
        pa[m] = *(const u32x4*)(pr + hs * 64 + m * 32);
        vs[2 * m] = *(const f32x4*)(vc + hs * 64 + m * 32); vs[2 * m + 1] = *(const f32x4*)(vc + hs * 64 + m * 32 + 4);
        vz[2 * m] = *(const f32x4*)(vc + NSTEP * 128 + hs * 64 + m * 32); vz[2 * m + 1] = *(const f32x4*)(vc + NSTEP * 128 + hs * 64 + m * 32 + 4);
      }
#pragma unroll
      for (int m = 0; m < 2; ++m) {
        asm volatile("" : "+v"(acc));
        if (m == 0) WAIT_FULL(4 * NSTEP + 2 * hs + 1);
        else if (hs == 2 * NSTEP - 1) { __builtin_amdgcn_s_barrier(); asm volatile("" ::: "memory"); }
        unsigned x[8];
#pragma unroll
        for (int j = 0; j < 8; ++j) x[j] = *(const unsigned*)(v0 + slot + m * 16384 + j * 1024);
        if (m == 1) {
          RELEASE(2 * NSTEP + hs + 1);
          asm volatile("" : "+v"(x[0]), "+v"(x[1]), "+v"(x[2]), "+v"(x[3]), "+v"(x[4]), "+v"(x[5]), "+v"(x[6]), "+v"(x[7]) :: "memory");
        }
        u32x4 b;
#pragma unroll
        for (int e = 0; e < 4; ++e)
          b[e] = deq2(x[2 * e], x[2 * e + 1], f32x2{vz[2 * m + (e >> 1)][2 * (e & 1)], vz[2 * m + (e >> 1)][2 * (e & 1) + 1]},
                      f32x2{vs[2 * m + (e >> 1)][2 * (e & 1)], vs[2 * m + (e >> 1)][2 * (e & 1) + 1]});
        acc = __builtin_amdgcn_mfma_f32_16x16x32_bf16(__builtin_bit_cast(bf16x8, pa[m]), __builtin_bit_cast(bf16x8, b), acc, 0, 0, 0);
      }
      if (hs == 2 * NSTEP - 1) break;
      NEXT_SLOT();
      if (hs == GATHER) {
#pragma unroll 1
        for (unsigned spins = 0; __builtin_amdgcn_ballot_w64(((ml[1] ^ tag) & ~paymask) != 0u) != 0ull && spins < (1u << 20); ++spins) {
          __builtin_amdgcn_s_sleep(8);
          asm volatile("" ::: "memory");
          ml = __builtin_amdgcn_raw_buffer_load_b64(rW, lane * 8, so_ml, AUX_SC1);
        }
        *(u32x2*)(lds + L_ML + (i >> 1) * 128 + (4 * wave + g) * 4 + hf * 2) = ml;
        MEET(4);
        ml = *(const u32x2*)(lds + L_ML + wave * 128 + lane * 2);
        const float f = out_factor(__uint_as_float(ml[0]), m_own);
        ebound = umax64(ml[1] & paymask);
        if (lane == 0) lds[L_FAC + wave] = f;
        MEET(5);
        __builtin_amdgcn_raw_buffer_store_b32(epoch + 1u, rW, (tid | split) == 0 ? 0 : OOB, bh * 256, AUX_SC1);
        fac = *(const f32x2*)(lds + L_FAC + head);
      }
      ml = __builtin_amdgcn_raw_buffer_load_b64(rW, hs == GATHER - 1 ? lane * 8 : OOB, so_ml, AUX_SC1);
    }
  }
  const int kexp = 151 - (int)ebound;
  const unsigned el = (bh * REP + head) * D + 16 * wave + i;
  unsigned long long* acc64 = (unsigned long long*)((char*)ws + WS_ACC) + el;
  const float o0 = (g >> 1 ? acc[2] : acc[0]) * fac[0], o1 = (g >> 1 ? acc[3] : acc[1]) * fac[1];
  const unsigned long long a0 = ((unsigned long long)(long long)__builtin_rint(__builtin_ldexp((double)o0, kexp)) << 6) + 1;
  const unsigned long long a1 = ((unsigned long long)(long long)__builtin_rint(__builtin_ldexp((double)o1, kexp)) << 6) + 1;
  const long long r0 = (long long)(__hip_atomic_fetch_add(acc64, a0, __ATOMIC_RELAXED, __HIP_MEMORY_SCOPE_AGENT) + a0);
  const long long r1 = (long long)(__hip_atomic_fetch_add(acc64 + D, a1, __ATOMIC_RELAXED, __HIP_MEMORY_SCOPE_AGENT) + a1);
  const f32x2 nanp = ebound == 255u ? f32x2{__uint_as_float(0x7fc00000u), __uint_as_float(0x7fc00000u)} : fac - fac;
  const unsigned pk = pk_bf16((float)__builtin_ldexp((double)(r0 >> 6), -kexp) + nanp[0], (float)__builtin_ldexp((double)(r1 >> 6), -kexp) + nanp[1]);
  __builtin_amdgcn_raw_buffer_store_b16((unsigned short)pk, rO, ((unsigned)r0 & 63u) == NSPLIT ? el * 2 : OOB, 0, 0);
  __builtin_amdgcn_raw_buffer_store_b16((unsigned short)(pk >> 16), rO, ((unsigned)r1 & 63u) == NSPLIT ? el * 2 + D * 2 : OOB, 0, 0);
}

extern "C" void kernel_launch(void* const* d_in, const int* in_sizes, int n_in, void* d_out, int out_size, void* d_ws, size_t ws_size,
                              hipStream_t stream) {
  kvq_decode_kernel<<<NBH * NSPLIT, (NWAVE + 1) * 64, 0, stream>>>((const unsigned short*)d_in[0], (const int*)d_in[1], (const int*)d_in[2],
                                                                   (const unsigned short*)d_in[3], (const unsigned short*)d_in[4],
                                                                   (const unsigned short*)d_in[5], (const unsigned short*)d_in[6],
                                                                   (unsigned*)d_ws, (unsigned short*)d_out);
}
```
